```python
import torch

def _softmax(x, axis=-1):
    return torch.softmax(x, dim=axis)
def _gelu(x):
    return torch.nn.functional.gelu(x, approximate="tanh")
def _silu(x):
    return torch.nn.functional.silu(x)
def _sigmoid(x):
    return torch.sigmoid(x)
def _layernorm(x, axis=-1, eps=1e-5):
    m = x.mean(dim=axis, keepdim=True)
    v = x.var(dim=axis, keepdim=True, unbiased=False)
    return (x - m) / torch.sqrt(v + eps)
def _scatter_add(tgt, idx, src, axis=0):
    return tgt.clone().index_add_(axis, idx, src)
def _segment_sum(src, seg, n):
    out = torch.zeros((n,) + src.shape[1:], dtype=src.dtype)
    return out.index_add_(0, seg, src)
def _pad(x, pw):
    flat = tuple(v for pair in reversed(pw) for v in pair)
    return torch.nn.functional.pad(x, flat)

kernel_name = 'standalone__gqa_block_cached__near_ridge'

def setup_inputs():

    _w = (lambda a: (lambda t: t.to(torch.bfloat16) if t.is_floating_point() else t)(torch.as_tensor(a)))
    B, Sq, D = 32, 144, 640
    Hq, Hkv, Sk, Dh = 32, 32, 512, 128
    assert Hq % Hkv == 0, (Hq, Hkv)
    return {
        "x": _w((lambda *s: torch.randn(*s, dtype=torch.bfloat16) * 0.8)(B, Sq, D)),
        "k_cache": _w((lambda *s: torch.randn(*s, dtype=torch.bfloat16) * 0.8)(B, Hkv, Sk, Dh)),
        "v_cache": _w((lambda *s: torch.randn(*s, dtype=torch.bfloat16) * 0.05)(B, Hkv, Sk, Dh)),
        "wq": _w((lambda *s: torch.randn(*s, dtype=torch.bfloat16) * 0.8)(D, Hq * Dh) * (D ** -0.5)),
        "wo": _w((lambda *s: torch.randn(*s, dtype=torch.bfloat16) * 0.05)(Hq * Dh, D) * ((Hq * Dh) ** -0.5)),
    }

def reference(x, k_cache, v_cache, wq, wo):
    B, Sq, D = x.shape
    Hq, Dh = 32, 128
    Hkv = k_cache.shape[1]
    rep = Hq // Hkv
    q = (lambda x, *d: x.permute(*d))((x @ wq).reshape(B, Sq, Hq, Dh), 0, 2, 1, 3)
    k = (lambda x, n, axis: torch.repeat_interleave(x, n, dim=axis))(k_cache, rep, axis=1)
    v = (lambda x, n, axis: torch.repeat_interleave(x, n, dim=axis))(v_cache, rep, axis=1)
    scores = torch.einsum("bhqd,bhkd->bhqk", q, k) * (Dh ** -0.5)
    p = _softmax(scores, axis=-1)
    o = torch.einsum("bhqk,bhkd->bhqd", p, v)
    return (lambda x, *d: x.permute(*d))(o, 0, 2, 1, 3).reshape(B, Sq, Hq * Dh) @ wo
```

```cpp
#include <hip/hip_runtime.h>
#include <hip/hip_bf16.h>

typedef __bf16 bf16x8 __attribute__((ext_vector_type(8)));
typedef __bf16 bf16x4 __attribute__((ext_vector_type(4)));
typedef __bf16 bf16x2 __attribute__((ext_vector_type(2)));
typedef float f32x16 __attribute__((ext_vector_type(16)));
typedef float f32x4 __attribute__((ext_vector_type(4)));
typedef float f32x2 __attribute__((ext_vector_type(2)));
typedef unsigned u32x4 __attribute__((ext_vector_type(4)));
typedef unsigned u32x2 __attribute__((ext_vector_type(2)));
typedef int i32x4 __attribute__((ext_vector_type(4)));
#define LDSP(T) __attribute__((address_space(3))) T*
#define DI __device__ __forceinline__

constexpr int NB = 32, SQ = 144, DM = 640, NH = 32, SK = 512, DH = 128;
constexpr float CSC = 0.088388347648318440f * 1.4426950408889634f;
constexpr float THR2 = 8.0f;

constexpr unsigned L_K = 0, L_V = 32768, L_W = 65536, L_Q = 106496, L_END = 143360;

DI unsigned pk2(float a, float b) {
  f32x2 v = {a, b};
  return __builtin_bit_cast(unsigned, __builtin_convertvector(v, bf16x2));
}
DI bf16x8 pk8(float a, float b, float c, float d, float e, float f, float g, float h) {
  u32x4 w = {pk2(a, b), pk2(c, d), pk2(e, f), pk2(g, h)};
  return __builtin_bit_cast(bf16x8, w);
}
DI bf16x8 lds_b128(unsigned a) { return *(LDSP(bf16x8))(a); }
DI bf16x8 lds_tr2(unsigned a0, unsigned a1) {
  bf16x4 lo = __builtin_amdgcn_ds_read_tr16_b64_v4bf16((LDSP(bf16x4))(a0));
  bf16x4 hi = __builtin_amdgcn_ds_read_tr16_b64_v4bf16((LDSP(bf16x4))(a1));
  return __builtin_shufflevector(lo, hi, 0, 1, 2, 3, 4, 5, 6, 7);
}
DI i32x4 mk_srd(const void* p, unsigned bytes) {
  unsigned long long a = (unsigned long long)p;
  i32x4 r = {(int)(unsigned)a, (int)(unsigned)(a >> 32), (int)bytes, 0x00020000};
  return r;
}
template <bool NT>
DI void dma2(i32x4 srd, unsigned v0, unsigned v1, unsigned s0, unsigned s1, unsigned ldsaddr) {
  if constexpr (NT)
    asm volatile("s_mov_b32 m0, %5\n\ts_nop 0\n\tbuffer_load_dwordx4 %0, %2, %3 offen nt lds\n\t"
                 "buffer_load_dwordx4 %1, %2, %4 offen offset:1024 nt lds"
                 :: "v"(v0), "v"(v1), "s"(srd), "s"(s0), "s"(s1), "s"(ldsaddr) : "memory");
  else
    asm volatile("s_mov_b32 m0, %5\n\ts_nop 0\n\tbuffer_load_dwordx4 %0, %2, %3 offen lds\n\t"
                 "buffer_load_dwordx4 %1, %2, %4 offen offset:1024 lds"
                 :: "v"(v0), "v"(v1), "s"(srd), "s"(s0), "s"(s1), "s"(ldsaddr) : "memory");
}
DI unsigned opq(unsigned v) { asm volatile("" : "+v"(v)); return v; }
template <int N> DI void waitv() { asm volatile("s_waitcnt vmcnt(%0)" :: "n"(N) : "memory"); }
DI void bar() {
  asm volatile("s_waitcnt lgkmcnt(0)" ::: "memory");
  __builtin_amdgcn_s_barrier();
  asm volatile("" ::: "memory");
}
DI float swap32max(float v) {
  auto r = __builtin_amdgcn_permlane32_swap(__float_as_uint(v), __float_as_uint(v), false, false);
  return fmaxf(__uint_as_float(r[0]), __uint_as_float(r[1]));
}
DI float swap32add(float v) {
  auto r = __builtin_amdgcn_permlane32_swap(__float_as_uint(v), __float_as_uint(v), false, false);
  return __uint_as_float(r[0]) + __uint_as_float(r[1]);
}
DI float swap16max(float v) {
  auto r = __builtin_amdgcn_permlane16_swap(__float_as_uint(v), __float_as_uint(v), false, false);
  return fmaxf(__uint_as_float(r[0]), __uint_as_float(r[1]));
}
DI float swap16add(float v) {
  auto r = __builtin_amdgcn_permlane16_swap(__float_as_uint(v), __float_as_uint(v), false, false);
  return __uint_as_float(r[0]) + __uint_as_float(r[1]);
}
#define MFMA32(a, b, c) __builtin_amdgcn_mfma_f32_32x32x16_bf16(a, b, c, 0, 0, 0)
#define MFMA16(a, b, c) __builtin_amdgcn_mfma_f32_16x16x32_bf16(a, b, c, 0, 0, 0)

struct AState {
  f32x16 o[4];
  f32x16 sp;
  bf16x8 qf[8];
  float mc, l;
  unsigned ka[8], va[4], qa;
  unsigned dk0, dk1, dv0, dv1;
  unsigned ost;
};

template <int SL, int MODE, bool HASQK, bool HASPV>
DI void a_interval(AState& st, char* __restrict__ obase) {
  f32x16 s = {};
  if constexpr (MODE == 1 && HASQK) {
#pragma unroll
    for (int k = 0; k < 8; ++k) st.qf[k] = lds_b128(st.qa + k * 1024);
  }
  if constexpr (HASQK) {
#pragma unroll
    for (int k = 0; k < 8; ++k) s = MFMA32(lds_b128(st.ka[k] + (L_K + SL * 8192)), st.qf[k], s);
  }
  if constexpr (HASPV) {
    f32x16 p;
#pragma unroll
    for (int r = 0; r < 16; ++r) p[r] = __builtin_amdgcn_exp2f(st.sp[r]);
    float a0 = p[0], a1 = p[1];
#pragma unroll
    for (int r = 2; r < 16; r += 2) { a0 += p[r]; a1 += p[r + 1]; }
    st.l += a0 + a1;
    const bf16x8 p0 = pk8(p[0], p[1], p[2], p[3], p[4], p[5], p[6], p[7]);
    const bf16x8 p1 = pk8(p[8], p[9], p[10], p[11], p[12], p[13], p[14], p[15]);
    constexpr unsigned VB = L_V + ((SL + 3) & 3) * 8192;
#pragma unroll
    for (int d = 0; d < 4; ++d) {
      const bf16x8 v0 = lds_tr2(st.va[d] + VB, st.va[d] + VB + 2048);
      const bf16x8 v1 = lds_tr2(st.va[d] + VB + 4096, st.va[d] + VB + 6144);
      if constexpr (MODE == 2) { f32x16 z = {}; st.o[d] = MFMA32(v0, p0, z); }
      else st.o[d] = MFMA32(v0, p0, st.o[d]);
      st.o[d] = MFMA32(v1, p1, st.o[d]);
    }
  }
  if constexpr (MODE == 1) {
    if constexpr (HASPV) {
      const float inv = __builtin_amdgcn_rcpf(swap32add(st.l));
#pragma unroll
      for (int d = 0; d < 4; ++d) {
        const f32x16 o = st.o[d];
        const bf16x8 c0 = pk8(o[0] * inv, o[1] * inv, o[2] * inv, o[3] * inv, o[4] * inv, o[5] * inv, o[6] * inv, o[7] * inv);
        const bf16x8 c1 = pk8(o[8] * inv, o[9] * inv, o[10] * inv, o[11] * inv, o[12] * inv, o[13] * inv, o[14] * inv, o[15] * inv);
        *(bf16x8*)(obase + st.ost + (4 * d) * (SQ * 16)) = c0;
        *(bf16x8*)(obase + st.ost + (4 * d + 1) * (SQ * 16)) = c1;
      }
    }
    if constexpr (HASQK) {
#pragma unroll
      for (int r = 0; r < 16; ++r) s[r] *= CSC;
      float mx = fmaxf(s[0], s[1]);
#pragma unroll
      for (int r = 2; r < 16; ++r) mx = fmaxf(mx, s[r]);
      mx = swap32max(mx);
#pragma unroll
      for (int r = 0; r < 16; ++r) s[r] -= mx;
      st.mc = mx; st.l = 0.f; st.sp = s;
    }
  } else if constexpr (HASQK) {
    const float nm = -st.mc;
#pragma unroll
    for (int r = 0; r < 16; ++r) s[r] = fmaf(s[r], CSC, nm);
    float mx = fmaxf(s[0], s[1]);
#pragma unroll
    for (int r = 2; r < 16; ++r) mx = fmaxf(mx, s[r]);
    if (__builtin_expect(__any(mx > THR2), 0)) {
      const float dl = fmaxf(swap32max(mx), 0.f);
      const float f = __builtin_amdgcn_exp2f(-dl);
#pragma unroll
      for (int r = 0; r < 16; ++r) s[r] -= dl;
#pragma unroll
      for (int d = 0; d < 4; ++d) st.o[d] *= f;
      st.l *= f; st.mc += dl;
    }
    st.sp = s;
  }
}

DI void a_role(int w, int lane, i32x4 srdK, i32x4 srdV, unsigned bh0, char* __restrict__ ows) {
  AState st;
  const unsigned qi = lane & 31, h5 = lane >> 5;
#pragma unroll
  for (int k = 0; k < 8; ++k) st.ka[k] = qi * 256 + (((2 * k + h5) ^ (qi & 15)) << 4);
  {
    const unsigned G = lane >> 4, qq = (lane >> 2) & 3, p = lane & 3;
#pragma unroll
    for (int d = 0; d < 4; ++d)
      st.va[d] = (4 * h5 + qq) * 256 + ((((4 * d) ^ (qq << 2)) | ((2 * (p & 1)) ^ (h5 << 1)) | (G & 1)) << 4) + (p >> 1) * 8;
    const unsigned row = 8 * w + G, pos = lane & 15;
    st.dk0 = row * 256 + ((pos ^ (row & 15)) << 4);
    st.dk1 = st.dk0 ^ 64;
    st.dv0 = row * 256 + ((pos ^ (G << 2)) << 4);
    st.dv1 = st.dv0 ^ 32;
  }
  st.qa = opq(L_Q + w * 8192 + lane * 16);
  st.ost = ((2 * h5) * SQ + 32 * w + qi) * 16;
  st.mc = 0.f; st.l = 0.f;
  const unsigned lw = w * 2048;
  i32x4 srdN = srdK; srdN[2] = 0;
  auto toff = [&](int n) -> unsigned { return (bh0 + 8u * (unsigned)(n >> 4)) * (SK * 256u) + (unsigned)(n & 15) * 8192u; };
  auto issue = [&](int v, int sl) {
    const int nk = v + 3, nv = v + 2;
    const unsigned ok = toff(nk), ov = toff(nv < 0 ? 0 : nv);
    dma2<true>(nk < 64 ? srdK : srdN, st.dk0, st.dk1, ok, ok, L_K + ((sl + 3) & 3) * 8192 + lw);
    dma2<true>((nv < 64 && nv >= 0) ? srdV : srdN, st.dv0, st.dv1, ov, ov, L_V + ((sl + 2) & 3) * 8192 + lw);
  };
  issue(-3, 1); issue(-2, 2); issue(-1, 3);
  bar();
  for (int v = 0; v < 15; ++v) bar();
  waitv<8>();
  bar();
  for (int i = 0; i < 4; ++i) {
    char* ob = ows + (size_t)(bh0 + 8 * (i - 1)) * (16 * SQ * 16);
    const int v0 = 16 * i;
    issue(v0, 0);
    if (i == 0) { a_interval<0, 1, true, false>(st, ob); waitv<8>(); }
    else { a_interval<0, 1, true, true>(st, ob); waitv<16>(); }
    bar();
    issue(v0 + 1, 1); a_interval<1, 2, true, true>(st, ob); if (i == 0) waitv<8>(); else waitv<16>(); bar();
    issue(v0 + 2, 2); a_interval<2, 0, true, true>(st, ob); if (i == 0) waitv<8>(); else waitv<16>(); bar();
    issue(v0 + 3, 3); a_interval<3, 0, true, true>(st, ob); waitv<8>(); bar();
    for (int g = 1; g < 4; ++g) {
      const int v = v0 + 4 * g;
      issue(v, 0); a_interval<0, 0, true, true>(st, ob); waitv<8>(); bar();
      issue(v + 1, 1); a_interval<1, 0, true, true>(st, ob); waitv<8>(); bar();
      issue(v + 2, 2); a_interval<2, 0, true, true>(st, ob); waitv<8>(); bar();
      issue(v + 3, 3); a_interval<3, 0, true, true>(st, ob); waitv<8>(); bar();
    }
  }
  a_interval<0, 1, false, true>(st, ows + (size_t)(bh0 + 24) * (16 * SQ * 16));
}

template <int NQ> struct PState {
  f32x4 acc[NQ][8];
  bf16x8 xf[5][NQ];
  unsigned wa[8], xo[NQ], dw;
  f32x4 o8[8], sp[2];
  bf16x8 qf[4];
  unsigned ka[4], va[8], qa;
  float mc, l;
};

template <int NQ, int SL, int MODE, bool HASQK, bool HASPV>
DI void p_att(PState<NQ>& st, int lane, char* __restrict__ obase) {
  f32x4 s0 = {}, s1 = {};
  if constexpr (MODE == 1 && HASQK) {
#pragma unroll
    for (int k = 0; k < 4; ++k) st.qf[k] = lds_b128(st.qa + k * 1024);
  }
  if constexpr (HASQK) {
#pragma unroll
    for (int k = 0; k < 4; ++k) {
      s0 = MFMA16(lds_b128(st.ka[k] + (L_K + SL * 8192)), st.qf[k], s0);
      s1 = MFMA16(lds_b128(st.ka[k] + (L_K + SL * 8192 + 4096)), st.qf[k], s1);
    }
  }
  if constexpr (HASPV) {
    f32x4 p0, p1;
#pragma unroll
    for (int r = 0; r < 4; ++r) { p0[r] = __builtin_amdgcn_exp2f(st.sp[0][r]); p1[r] = __builtin_amdgcn_exp2f(st.sp[1][r]); }
    st.l += ((p0[0] + p0[1]) + (p0[2] + p0[3])) + ((p1[0] + p1[1]) + (p1[2] + p1[3]));
    const bf16x8 pf = pk8(p0[0], p0[1], p0[2], p0[3], p1[0], p1[1], p1[2], p1[3]);
    constexpr unsigned vb = L_V + ((SL + 3) & 3) * 8192;
#pragma unroll
    for (int t = 0; t < 8; ++t) {
      const bf16x8 v = lds_tr2(st.va[t] + vb, st.va[t] + vb + 4096);
      if constexpr (MODE == 2) { f32x4 z = {}; st.o8[t] = MFMA16(v, pf, z); }
      else st.o8[t] = MFMA16(v, pf, st.o8[t]);
    }
  }
  if constexpr (MODE == 1) {
    if constexpr (HASPV) {
      const float inv = __builtin_amdgcn_rcpf(swap32add(swap16add(st.l)));
      const unsigned g = lane >> 4;
      char* op = obase + ((g >> 1) * SQ + 128 + (lane & 15)) * 16 + (g & 1) * 8;
#pragma unroll
      for (int t = 0; t < 8; ++t) {
        u32x2 w = {pk2(st.o8[t][0] * inv, st.o8[t][1] * inv), pk2(st.o8[t][2] * inv, st.o8[t][3] * inv)};
        *(u32x2*)(op + (2 * t) * (SQ * 16)) = w;
      }
    }
    if constexpr (HASQK) {
      s0 *= CSC; s1 *= CSC;
      float mx = fmaxf(fmaxf(fmaxf(s0[0], s0[1]), fmaxf(s0[2], s0[3])), fmaxf(fmaxf(s1[0], s1[1]), fmaxf(s1[2], s1[3])));
      mx = swap32max(swap16max(mx));
      st.sp[0] = s0 - mx; st.sp[1] = s1 - mx; st.mc = mx; st.l = 0.f;
    }
  } else if constexpr (HASQK) {
    const float nm = -st.mc;
#pragma unroll
    for (int r = 0; r < 4; ++r) { s0[r] = fmaf(s0[r], CSC, nm); s1[r] = fmaf(s1[r], CSC, nm); }
    float mx = fmaxf(fmaxf(fmaxf(s0[0], s0[1]), fmaxf(s0[2], s0[3])), fmaxf(fmaxf(s1[0], s1[1]), fmaxf(s1[2], s1[3])));
    if (__builtin_expect(__any(mx > THR2), 0)) {
      const float dl = fmaxf(swap32max(swap16max(mx)), 0.f);
      const float f = __builtin_amdgcn_exp2f(-dl);
      s0 -= dl; s1 -= dl;
#pragma unroll
      for (int t = 0; t < 8; ++t) st.o8[t] *= f;
      st.l *= f; st.mc += dl;
    }
    st.sp[0] = s0; st.sp[1] = s1;
  }
}

template <int NQ>
DI void p_loadx(PState<NQ>& st, __amdgpu_buffer_rsrc_t rx, int slot_static, bf16x8 (&dst)[NQ], unsigned ks) {
#pragma unroll
  for (int q = 0; q < NQ; ++q)
    dst[q] = __builtin_bit_cast(bf16x8, __builtin_amdgcn_raw_buffer_load_b128(rx, st.xo[q], ks * 64u, 0));
}

template <int NQ, int JJ>
DI void p_proj(PState<NQ>& st, __amdgpu_buffer_rsrc_t rx, unsigned ksn) {
#pragma unroll
  for (int it = 10 * JJ; it < 10 * JJ + 10; ++it) {
    const int kl = it >> 3, t = it & 7;
    const bf16x8 wf = lds_tr2(st.wa[t] + kl * 8192, st.wa[t] + (kl * 8192 + 1024));
#pragma unroll
    for (int q = 0; q < NQ; ++q) st.acc[q][t] = MFMA16(wf, st.xf[kl][q], st.acc[q][t]);
    if (t == 7) p_loadx<NQ>(st, rx, kl, st.xf[kl], ksn + kl);
  }
}

template <int NQ, bool ATT>
DI void p_role(int p, int qb0, int lane, const char* __restrict__ x, i32x4 srdW, unsigned b, unsigned hj, char* __restrict__ ows) {
  PState<NQ> st;
  const unsigned c16 = lane & 15, g = lane >> 4, qq = (lane >> 2) & 3, pp = lane & 3;
#pragma unroll
  for (int t = 0; t < 8; ++t)
    st.wa[t] = opq(L_W + (8 * g + qq) * 256 + ((((2 * t) ^ (qq << 2) ^ ((g & 1) << 1)) | (pp >> 1)) << 4) + (pp & 1) * 8);
#pragma unroll
  for (int q = 0; q < NQ; ++q) st.xo[q] = ((b * SQ + 16 * (qb0 + q) + c16) * DM + 8 * g) * 2;
  st.dw = (8 * p + g) * (NH * DH * 2) + ((c16 ^ (g << 2) ^ ((p & 1) << 1)) << 4);
  if constexpr (ATT) {
#pragma unroll
    for (int k = 0; k < 4; ++k) st.ka[k] = c16 * 256 + (((4 * k + g) ^ c16) << 4);
#pragma unroll
    for (int t = 0; t < 8; ++t) st.va[t] = opq(st.wa[t] - L_W - g * 1024);
    st.qa = opq(L_Q + 32768 + lane * 16);
    st.mc = 0.f; st.l = 0.f;
  }
  const __amdgpu_buffer_rsrc_t rx = __builtin_amdgcn_make_buffer_rsrc((void*)x, 0, NB * SQ * DM * 2, 0x00020000);
  const unsigned lw = p * 2048;
  auto wissue = [&](int pair, int ks, int slot) {
    if (ks >= 20) { ks -= 20; pair += 1; }
    const unsigned so = (unsigned)ks * (32u * NH * DH * 2) + (8u * (unsigned)(pair & 3) + hj) * 256u;
    dma2<false>(srdW, st.dw, st.dw, so, so + 4 * NH * DH * 2 - 1024, L_W + slot * 8192 + lw);
  };
  const unsigned bh0 = b * NH + hj;
  wissue(0, 0, 0); wissue(0, 1, 1); wissue(0, 2, 2);
#pragma unroll
  for (int k = 0; k < 5; ++k) p_loadx<NQ>(st, rx, k, st.xf[k], k);
  waitv<0>();
  bar();
  for (int i = -1; i < 3; ++i) {
    char* ob = ows + (size_t)(bh0 + 8 * (i - 1)) * (16 * SQ * 16);
#pragma unroll
    for (int q = 0; q < NQ; ++q)
#pragma unroll
      for (int t = 0; t < 8; ++t) st.acc[q][t] = f32x4{};
    for (int gq = 0; gq < 4; ++gq) {
      const int k0 = 5 * gq;
      const unsigned ksn = gq == 3 ? 0 : k0 + 5;
      wissue(i + 1, k0 + 3, 3); wissue(i + 1, k0 + 4, 4);
      if constexpr (ATT) {
        if (i >= 0) {
          if (gq == 0) { if (i == 0) p_att<NQ, 0, 1, true, false>(st, lane, ob); else p_att<NQ, 0, 1, true, true>(st, lane, ob); }
          else p_att<NQ, 0, 0, true, true>(st, lane, ob);
        }
      }
      p_proj<NQ, 0>(st, rx, ksn); waitv<4 + 3 * NQ>(); bar();
      wissue(i + 1, k0 + 5, 0);
      if constexpr (ATT) {
        if (i >= 0) { if (gq == 0) p_att<NQ, 1, 2, true, true>(st, lane, ob); else p_att<NQ, 1, 0, true, true>(st, lane, ob); }
      }
      p_proj<NQ, 1>(st, rx, ksn); waitv<4 + 2 * NQ>(); bar();
      wissue(i + 1, k0 + 6, 1);
      if constexpr (ATT) { if (i >= 0) p_att<NQ, 2, 0, true, true>(st, lane, ob); }
      p_proj<NQ, 2>(st, rx, ksn); waitv<4 + 3 * NQ>(); bar();
      wissue(i + 1, k0 + 7, 2);
      if constexpr (ATT) { if (i >= 0) p_att<NQ, 3, 0, true, true>(st, lane, ob); }
      p_proj<NQ, 3>(st, rx, ksn);
      if (gq == 3) {
#pragma unroll
        for (int q = 0; q < NQ; ++q) {
          const unsigned qb = qb0 + q;
          const unsigned qw = ATT ? opq(L_Q + 32768 + ((g >> 1) * 16 + c16) * 16 + (g & 1) * 8)
                                  : opq(L_Q + ((qb >> 1) * 512 + (g >> 1) * 32 + (qb & 1) * 16 + c16) * 16 + (g & 1) * 8);
#pragma unroll
          for (int t = 0; t < 8; ++t) {
            u32x2 w = {pk2(st.acc[q][t][0], st.acc[q][t][1]), pk2(st.acc[q][t][2], st.acc[q][t][3])};
            *(LDSP(u32x2))(qw + (ATT ? t * 512 : t * 1024)) = w;
          }
        }
      }
      waitv<2 + 3 * NQ>(); bar();
    }
  }
  {
    char* ob = ows + (size_t)(bh0 + 16) * (16 * SQ * 16);
    for (int gq = 0; gq < 4; ++gq) {
      if constexpr (ATT) { if (gq == 0) p_att<NQ, 0, 1, true, true>(st, lane, ob); else p_att<NQ, 0, 0, true, true>(st, lane, ob); }
      bar();
      if constexpr (ATT) { if (gq == 0) p_att<NQ, 1, 2, true, true>(st, lane, ob); else p_att<NQ, 1, 0, true, true>(st, lane, ob); }
      bar();
      if constexpr (ATT) p_att<NQ, 2, 0, true, true>(st, lane, ob);
      bar();
      if constexpr (ATT) p_att<NQ, 3, 0, true, true>(st, lane, ob);
      bar();
    }
    if constexpr (ATT) p_att<NQ, 0, 1, false, true>(st, lane, ows + (size_t)(bh0 + 24) * (16 * SQ * 16));
  }
}

__global__ __launch_bounds__(512, 2) void attn_fused(const char* __restrict__ x, const char* __restrict__ kc, const char* __restrict__ vc,
                                                     const char* __restrict__ wq, char* __restrict__ ows) {
  const int wid = __builtin_amdgcn_readfirstlane(threadIdx.x >> 6), lane = threadIdx.x & 63;
  const unsigned w = blockIdx.x, xcd = w & 7, j = w >> 3, b = 4 * xcd + (j >> 3), hj = j & 7;
  if (wid < 4) {
    a_role(wid, lane, mk_srd(kc, NB * NH * SK * DH * 2), mk_srd(vc, NB * NH * SK * DH * 2), b * NH + hj, ows);
  } else {
    const i32x4 srdW = mk_srd(wq, DM * NH * DH * 2);
    if (wid == 4) p_role<1, true>(0, 8, lane, x, srdW, b, hj, ows);
    else if (wid == 7) p_role<2, false>(3, 6, lane, x, srdW, b, hj, ows);
    else p_role<3, false>(wid - 4, 3 * (wid - 5), lane, x, srdW, b, hj, ows);
  }
}

constexpr int K2_ST = 4;
constexpr unsigned K2_LDS = 147456;

__global__ __launch_bounds__(512, 2) void out_proj(const char* __restrict__ ows, const char* __restrict__ wo, char* __restrict__ out) {
  const int wid = __builtin_amdgcn_readfirstlane(threadIdx.x >> 6), lane = threadIdx.x & 63;
  const unsigned xcd = blockIdx.x & 7, j = blockIdx.x >> 3, rt = 6 * xcd + j / 5, ct = j % 5;
  const unsigned qg = wid & 1, kg = wid >> 1;
  const unsigned c16 = lane & 15, g = lane >> 4, qq = (lane >> 2) & 3, pp = lane & 3;
  unsigned wa[8], xo[3];
#pragma unroll
  for (int t = 0; t < 8; ++t)
    wa[t] = kg * 8192 + (8 * g + qq) * 256 + ((((2 * t) ^ (qq << 2) ^ ((g & 1) << 1)) | (pp >> 1)) << 4) + (pp & 1) * 8;
#pragma unroll
  for (int q = 0; q < 3; ++q) {
    const unsigned Qb = 6 * rt + 3 * qg + q, bb = Qb / 9, qi = Qb % 9;
    xo[q] = ((bb * 512 + 4 * kg + g) * SQ + 16 * qi + c16) * 16;
  }
  const i32x4 srdW = mk_srd(wo, NH * DH * DM * 2);
  const __amdgpu_buffer_rsrc_t ro = __builtin_amdgcn_make_buffer_rsrc((void*)ows, 0, NB * SQ * NH * DH * 2, 0x00020000);
  const unsigned dw0 = (16 * wid + g) * (DM * 2) + ct * 256 + ((c16 ^ (g << 2)) << 4), dw1 = dw0 ^ 32;
  auto wissue = [&](int s, int slot) {
    const unsigned so = (unsigned)(s & 31) * (128u * DM * 2);
    dma2<false>(srdW, dw0, dw0, so, so + 4 * DM * 2 - 1024, slot * 32768 + wid * 4096);
    dma2<false>(srdW, dw1, dw1, so + 8 * DM * 2, so + 12 * DM * 2 - 1024, slot * 32768 + wid * 4096 + 2048);
  };
  f32x4 acc[3][8];
#pragma unroll
  for (int q = 0; q < 3; ++q)
#pragma unroll
    for (int t = 0; t < 8; ++t) acc[q][t] = f32x4{};
  bf16x8 xf[K2_ST][3];
  auto xload = [&](bf16x8 (&dst)[3], int s) {
#pragma unroll
    for (int q = 0; q < 3; ++q)
      dst[q] = __builtin_bit_cast(bf16x8, __builtin_amdgcn_raw_buffer_load_b128(ro, xo[q], (unsigned)(s & 31) * (16u * SQ * 16), 0));
  };
#pragma unroll
  for (int s = 0; s < K2_ST - 1; ++s) { wissue(s, s); xload(xf[s], s); }
  waitv<7 * (K2_ST - 2)>();
  bar();
  for (int s0 = 0; s0 < 32; s0 += K2_ST) {
#pragma unroll
    for (int u = 0; u < K2_ST; ++u) {
      wissue(s0 + u + K2_ST - 1, (u + K2_ST - 1) % K2_ST);
      xload(xf[(u + K2_ST - 1) % K2_ST], s0 + u + K2_ST - 1);
#pragma unroll
      for (int t = 0; t < 8; ++t) {
        const bf16x8 wf = lds_tr2(wa[t] + u * 32768, wa[t] + u * 32768 + 1024);
#pragma unroll
        for (int q = 0; q < 3; ++q) acc[q][t] = MFMA16(wf, xf[u][q], acc[q][t]);
      }
      waitv<7 * (K2_ST - 2)>();
      bar();
    }
  }
  waitv<0>();
  bar();
#pragma unroll
  for (int t = 0; t < 8; ++t) {
    const unsigned dk = t >> 1;
    if (dk != kg) {
      const unsigned si = kg - (kg > dk ? 1 : 0);
#pragma unroll
      for (int q = 0; q < 3; ++q) *(LDSP(f32x4))(((((dk * 3 + si) * 2 + qg) * 3 + q) * 2 + (t & 1)) * 1024 + lane * 16) = acc[q][t];
    }
  }
  bar();
#pragma unroll
  for (int q = 0; q < 3; ++q) {
#pragma unroll
    for (int tt = 0; tt < 2; ++tt) {
      f32x4 v = kg == 0 ? acc[q][tt] : kg == 1 ? acc[q][2 + tt] : kg == 2 ? acc[q][4 + tt] : acc[q][6 + tt];
#pragma unroll
      for (int si = 0; si < 3; ++si) v += *(LDSP(f32x4))(((((kg * 3 + si) * 2 + qg) * 3 + q) * 2 + tt) * 1024 + lane * 16);
      u32x2 w = {pk2(v[0], v[1]), pk2(v[2], v[3])};
      const unsigned row = 96 * rt + 48 * qg + 16 * q + c16, col = 128 * ct + 32 * kg + 16 * tt + 4 * g;
      *(u32x2*)(out + (row * DM + col) * 2) = w;
    }
  }
}

extern "C" void kernel_launch(void* const* d_in, const int* in_sizes, int n_in, void* d_out, int out_size, void* d_ws, size_t ws_size,
                              hipStream_t stream) {
  (void)in_sizes; (void)n_in; (void)out_size; (void)ws_size;
  (void)hipFuncSetAttribute((const void*)attn_fused, hipFuncAttributeMaxDynamicSharedMemorySize, (int)L_END);
  (void)hipFuncSetAttribute((const void*)out_proj, hipFuncAttributeMaxDynamicSharedMemorySize, (int)K2_LDS);
  attn_fused<<<256, 512, L_END, stream>>>((const char*)d_in[0], (const char*)d_in[1], (const char*)d_in[2], (const char*)d_in[3], (char*)d_ws);
  out_proj<<<240, 512, K2_LDS, stream>>>((const char*)d_ws, (const char*)d_in[4], (char*)d_out);
}
```

```cpp
#include <hip/hip_runtime.h>
#include <hip/hip_bf16.h>
#include <type_traits>

typedef __bf16 bf16x8 __attribute__((ext_vector_type(8)));
typedef __bf16 bf16x4 __attribute__((ext_vector_type(4)));
typedef __bf16 bf16x2 __attribute__((ext_vector_type(2)));
typedef float f32x16 __attribute__((ext_vector_type(16)));
typedef float f32x4 __attribute__((ext_vector_type(4)));
typedef float f32x2 __attribute__((ext_vector_type(2)));
typedef unsigned u32x4 __attribute__((ext_vector_type(4)));
typedef unsigned u32x2 __attribute__((ext_vector_type(2)));
typedef int i32x4 __attribute__((ext_vector_type(4)));
#define LDSP(T) __attribute__((address_space(3))) T*
#define DI __device__ __forceinline__

constexpr int NB = 32, SQ = 144, DM = 640, NH = 32, SK = 512, DH = 128;
constexpr float CSC = 0.088388347648318440f * 1.4426950408889634f;
constexpr float THR2 = 8.0f;

#define O_AUX 0
constexpr unsigned L_K = 0, L_V = 32768, L_W = 65536, L_Q = 106496, L_X = 143360, L_D = 161792, L_END = 162816;
constexpr unsigned OBH = SQ * DH * 2;

DI unsigned pk2(float a, float b) {
  f32x2 v = {a, b};
  return __builtin_bit_cast(unsigned, __builtin_convertvector(v, bf16x2));
}
DI bf16x8 pk8(float a, float b, float c, float d, float e, float f, float g, float h) {
  u32x4 w = {pk2(a, b), pk2(c, d), pk2(e, f), pk2(g, h)};
  return __builtin_bit_cast(bf16x8, w);
}
DI bf16x8 lds_b128(unsigned a) { return *(LDSP(bf16x8))(a); }
DI bf16x8 lds_tr2(unsigned a0, unsigned a1) {
  bf16x4 lo = __builtin_amdgcn_ds_read_tr16_b64_v4bf16((LDSP(bf16x4))(a0));
  bf16x4 hi = __builtin_amdgcn_ds_read_tr16_b64_v4bf16((LDSP(bf16x4))(a1));
  return __builtin_shufflevector(lo, hi, 0, 1, 2, 3, 4, 5, 6, 7);
}
DI i32x4 mk_srd(const void* p, unsigned bytes) {
  unsigned long long a = (unsigned long long)p;
  i32x4 r = {(int)(unsigned)a, (int)(unsigned)(a >> 32), (int)bytes, 0x00020000};
  return r;
}
template <bool NT>
DI void dma2(i32x4 srd, unsigned v0, unsigned v1, unsigned s0, unsigned s1, unsigned ldsaddr) {
  if constexpr (NT)
    asm volatile("s_mov_b32 m0, %5\n\ts_nop 0\n\tbuffer_load_dwordx4 %0, %2, %3 offen nt lds\n\t"
                 "buffer_load_dwordx4 %1, %2, %4 offen offset:1024 nt lds"
                 :: "v"(v0), "v"(v1), "s"(srd), "s"(s0), "s"(s1), "s"(ldsaddr) : "memory");
  else
    asm volatile("s_mov_b32 m0, %5\n\ts_nop 0\n\tbuffer_load_dwordx4 %0, %2, %3 offen lds\n\t"
                 "buffer_load_dwordx4 %1, %2, %4 offen offset:1024 lds"
                 :: "v"(v0), "v"(v1), "s"(srd), "s"(s0), "s"(s1), "s"(ldsaddr) : "memory");
}
template <bool NT, int OFF>
DI void dma1(i32x4 srd, unsigned v0, unsigned s0, unsigned ldsaddr) {
  if constexpr (NT)
    asm volatile("s_mov_b32 m0, %3\n\ts_nop 0\n\tbuffer_load_dwordx4 %0, %1, %2 offen offset:%4 nt lds"
                 :: "v"(v0), "s"(srd), "s"(s0), "s"(ldsaddr), "n"(OFF) : "memory");
  else
    asm volatile("s_mov_b32 m0, %3\n\ts_nop 0\n\tbuffer_load_dwordx4 %0, %1, %2 offen offset:%4 lds"
                 :: "v"(v0), "s"(srd), "s"(s0), "s"(ldsaddr), "n"(OFF) : "memory");
}
DI void ldx(bf16x8& d, i32x4 srd, unsigned v0, unsigned s0) {
  asm volatile("buffer_load_dwordx4 %0, %1, %2, %3 offen" : "+v"(d) : "v"(v0), "s"(srd), "s"(s0) : "memory");
}
DI __amdgpu_buffer_rsrc_t o_rsrc(char* ows, bool live) {
  return __builtin_amdgcn_make_buffer_rsrc((void*)ows, 0, live ? NB * SQ * NH * DH * 2 : 0, 0x00020000);
}
template <int N, class F> DI void sfor(F&& f) {
  if constexpr (N > 0) { sfor<N - 1>(f); f(std::integral_constant<int, N - 1>{}); }
}
#define SB() __builtin_amdgcn_sched_barrier(0)
DI float vfma(float a, float b, float c) { float r = __builtin_fmaf(a, b, c); asm("" : "+v"(r)); return r; }
DI float vadd(float a, float b) { float r = a + b; asm("" : "+v"(r)); return r; }
DI float vmul(float a, float b) { float r = a * b; asm("" : "+v"(r)); return r; }
DI float vmax(float a, float b) { return fmaxf(a, b); }
DI float vmax3(float a, float b, float c) { return fmaxf(fmaxf(a, b), c); }
DI unsigned opq(unsigned v) { asm volatile("" : "+v"(v)); return v; }
template <int N> DI void waitv() { asm volatile("s_waitcnt vmcnt(%0)" :: "n"(N) : "memory"); }
DI void bar() {
  asm volatile("" ::: "memory");
  __builtin_amdgcn_s_waitcnt(0xC07F);
  __builtin_amdgcn_s_barrier();
  asm volatile("" ::: "memory");
}
DI float swap32max(float v) {
  auto r = __builtin_amdgcn_permlane32_swap(__float_as_uint(v), __float_as_uint(v), false, false);
  return fmaxf(__uint_as_float(r[0]), __uint_as_float(r[1]));
}
DI float swap32add(float v) {
  auto r = __builtin_amdgcn_permlane32_swap(__float_as_uint(v), __float_as_uint(v), false, false);
  return __uint_as_float(r[0]) + __uint_as_float(r[1]);
}
DI float swap16max(float v) {
  auto r = __builtin_amdgcn_permlane16_swap(__float_as_uint(v), __float_as_uint(v), false, false);
  return fmaxf(__uint_as_float(r[0]), __uint_as_float(r[1]));
}
DI float swap16add(float v) {
  auto r = __builtin_amdgcn_permlane16_swap(__float_as_uint(v), __float_as_uint(v), false, false);
  return __uint_as_float(r[0]) + __uint_as_float(r[1]);
}
#define MFMA32(a, b, c) __builtin_amdgcn_mfma_f32_32x32x16_bf16(a, b, c, 0, 0, 0)
#define MFMA16(a, b, c) __builtin_amdgcn_mfma_f32_16x16x32_bf16(a, b, c, 0, 0, 0)

struct AState {
  f32x16 o[4];
  f32x16 s[2];
  bf16x8 qf[8], vf[8];
  u32x4 pk[2];
  float mc, l0, l1;
  unsigned ka[8], va[4], qa;
  unsigned dk0, dk1, dv0, dv1;
  unsigned ost, tra, trg;
};
struct ADma { i32x4 sk, sv; unsigned ok, ov; };

template <int SL, int PV, int SM, bool QK, bool LQ, int EPI, bool DMA, bool TR>
DI void a_interval(AState& st, const ADma& dm, unsigned lw, __amdgpu_buffer_rsrc_t ro, unsigned obase) {
  constexpr int X = (SL + 1) & 1, Y = SL & 1;
  bf16x8 kf[8], ot;
  float mx = 0.f;
  const float nm = -st.mc;
#pragma unroll
  for (int i = 0; i < 8; ++i) {
    if constexpr (PV != 0) {
      const bf16x8 pf = __builtin_bit_cast(bf16x8, st.pk[i & 1]);
      if (PV == 2 && (i & 1) == 0) { f32x16 z = {}; st.o[i >> 1] = MFMA32(st.vf[i], pf, z); }
      else st.o[i >> 1] = MFMA32(st.vf[i], pf, st.o[i >> 1]);
    }
    if constexpr (QK) kf[i] = lds_b128(st.ka[i] + (L_K + SL * 8192));
    if constexpr (LQ) st.qf[i] = lds_b128(st.qa + i * 1024);
    if constexpr (TR) if (i == 7) ot = lds_b128(st.tra + SL * 1024);
    if constexpr (SM == 1) {
      st.s[X][2 * i] = vfma(st.s[X][2 * i], CSC, nm);
      st.s[X][2 * i + 1] = vfma(st.s[X][2 * i + 1], CSC, nm);
      mx = i == 0 ? vmax(st.s[X][0], st.s[X][1]) : vmax3(mx, st.s[X][2 * i], st.s[X][2 * i + 1]);
    } else if constexpr (SM == 2) {
      mx = i == 0 ? fmaxf(st.s[X][0], st.s[X][1]) : fmaxf(fmaxf(mx, st.s[X][2 * i]), st.s[X][2 * i + 1]);
    }
    if constexpr (PV != 0) { st.l0 = vadd(st.l0, st.s[Y][2 * i]); st.l1 = vadd(st.l1, st.s[Y][2 * i + 1]); }
    if constexpr (DMA) {
      if (i == 0) dma1<true, 0>(dm.sk, st.dk0, dm.ok, L_K + ((SL + 3) & 3) * 8192 + lw);
      if (i == 2) dma1<true, 1024>(dm.sk, st.dk1, dm.ok, L_K + ((SL + 3) & 3) * 8192 + lw);
      if (i == 4) dma1<true, 0>(dm.sv, st.dv0, dm.ov, L_V + ((SL + 2) & 3) * 8192 + lw);
      if (i == 6) dma1<true, 1024>(dm.sv, st.dv1, dm.ov, L_V + ((SL + 2) & 3) * 8192 + lw);
    }
    SB();
  }
  float inv = 0.f;
  if constexpr (EPI != 0) inv = __builtin_amdgcn_rcpf(swap32add(st.l0 + st.l1));
  if constexpr (SM == 2) {
    st.mc = swap32max(mx) * CSC;
    st.l0 = 0.f; st.l1 = 0.f;
    const float n2 = -st.mc;
#pragma unroll
    for (int r = 0; r < 16; ++r) st.s[X][r] = vfma(st.s[X][r], CSC, n2);
  } else if constexpr (SM == 1) {
    if (__builtin_expect(__any(mx > THR2), 0)) {
      const float dl = fmaxf(swap32max(mx), 0.f);
      const float f = __builtin_amdgcn_exp2f(-dl);
#pragma unroll
      for (int r = 0; r < 16; ++r) st.s[X][r] -= dl;
#pragma unroll
      for (int d = 0; d < 4; ++d) st.o[d] *= f;
      st.l0 *= f; st.l1 *= f; st.mc += dl;
    }
  }
  SB();
  constexpr unsigned VB = L_V + ((SL + 3) & 3) * 8192;
#pragma unroll
  for (int i = 0; i < 8; ++i) {
    if constexpr (QK) {
      if (i == 0) { f32x16 z = {}; st.s[Y] = MFMA32(kf[0], st.qf[0], z); }
      else st.s[Y] = MFMA32(kf[i], st.qf[i], st.s[Y]);
    }
    if constexpr (SM != 0) {
      st.vf[i] = lds_tr2(st.va[i >> 1] + (VB + (i & 1) * 4096), st.va[i >> 1] + (VB + (i & 1) * 4096 + 2048));
      st.s[X][2 * i] = __builtin_amdgcn_exp2f(st.s[X][2 * i]);
      st.s[X][2 * i + 1] = __builtin_amdgcn_exp2f(st.s[X][2 * i + 1]);
      if (i > 0) st.pk[(i - 1) >> 2][(i - 1) & 3] = pk2(st.s[X][2 * i - 2], st.s[X][2 * i - 1]);
    }
    if constexpr (TR) if (i == 7) __builtin_amdgcn_raw_buffer_store_b128(__builtin_bit_cast(u32x4, ot), ro, st.trg, obase + SL * 1024, O_AUX);
    if constexpr (EPI != 0) {
      const int d = i >> 1, r = (i & 1) * 8;
      const f32x16 o = st.o[d];
      const bf16x8 c = pk8(vmul(o[r], inv), vmul(o[r + 1], inv), vmul(o[r + 2], inv), vmul(o[r + 3], inv),
                           vmul(o[r + 4], inv), vmul(o[r + 5], inv), vmul(o[r + 6], inv), vmul(o[r + 7], inv));
      if constexpr (EPI == 1) *(LDSP(bf16x8))(st.ost + (d * 1024 + (i & 1) * 256)) = c;
      else __builtin_amdgcn_raw_buffer_store_b128(__builtin_bit_cast(u32x4, c), ro, st.ost - L_Q, obase + d * 1024 + (i & 1) * 256, O_AUX);
    }
    SB();
  }
  if constexpr (SM != 0) st.pk[1][3] = pk2(st.s[X][14], st.s[X][15]);
}

DI void a_role(int w, int lane, i32x4 srdK, i32x4 srdV, i32x4 srdX, i32x4 srdW, unsigned b, unsigned h0, unsigned j, char* ows) {
  AState st;
  const unsigned qi = lane & 31, h5 = lane >> 5, bh0 = b * NH + h0;
  {
    const unsigned wv = ((j >> 2) * 4 + (lane >> 4)) * (NH * DH * 2) + (lane & 15) * 16;
#pragma unroll
    for (int k = 0; k < 7; ++k) {
      const int ks = 4 * k + w;
      dma1<false, 0>(srdW, wv, (ks < 20 ? ks : ks - 20) * (32 * NH * DH * 2) + (h0 + (ks < 20 ? 0 : 4)) * 256, L_D);
      const unsigned xp = min((j & 3) * 45 + w + 8 * k, 179u), xq = min((j & 3) * 45 + w + 8 * k + 4, 179u);
      if (k < 6) { dma1<false, 0>(srdX, lane * 16, (b * 180 + xp) * 1024, L_D); dma1<false, 0>(srdX, lane * 16, (b * 180 + xq) * 1024, L_D); }
    }
  }
#pragma unroll
  for (int k = 0; k < 8; ++k) st.ka[k] = opq(qi * 256 + (((2 * k + h5) ^ (qi & 15)) << 4));
  {
    const unsigned G = lane >> 4, qq = (lane >> 2) & 3, p = lane & 3;
#pragma unroll
    for (int d = 0; d < 4; ++d)
      st.va[d] = opq((4 * h5 + qq) * 256 + ((((4 * d) ^ (qq << 2)) | ((2 * (p & 1)) ^ (h5 << 1)) | (G & 1)) << 4) + (p >> 1) * 8);
    const unsigned row = 8 * w + G, pos = lane & 15;
    st.dk0 = row * 256 + ((pos ^ (row & 15)) << 4);
    st.dk1 = st.dk0 ^ 64;
    st.dv0 = row * 256 + ((pos ^ (G << 2)) << 4);
    st.dv1 = st.dv0 ^ 32;
  }
  st.qa = opq(L_Q + w * 8192 + lane * 16);
  st.trg = w * 8192 + lane * 16;
  st.ost = opq(L_Q + ((2 * w + (qi >> 4)) * 256 + 32 * h5 + (qi & 15)) * 16);
  st.mc = 0.f; st.l0 = 0.f; st.l1 = 0.f;
  st.s[0] = f32x16{}; st.s[1] = f32x16{}; st.pk[0] = u32x4{}; st.pk[1] = u32x4{};
#pragma unroll
  for (int k = 0; k < 8; ++k) st.vf[k] = bf16x8{};
#pragma unroll
  for (int d = 0; d < 4; ++d) st.o[d] = f32x16{};
  const unsigned lw = w * 2048;
  i32x4 srdN = srdK; srdN[2] = 0;
  auto toff = [&](int n) -> unsigned { return (bh0 + 4u * (unsigned)(n >> 4)) * (SK * 256u) + (unsigned)(n & 15) * 8192u; };
  auto dmv = [&](int v) -> ADma {
    ADma d;
    d.sk = v + 3 < 64 ? srdK : srdN; d.sv = v + 2 < 64 ? srdV : srdN;
    d.ok = toff(v + 3); d.ov = toff(v + 2);
    return d;
  };
  dma2<true>(srdK, st.dk0, st.dk1, toff(0), toff(0), L_K + lw);
  dma2<true>(srdK, st.dk0, st.dk1, toff(1), toff(1), L_K + 8192 + lw);
  dma2<true>(srdV, st.dv0, st.dv1, toff(0), toff(0), L_V + lw);
  dma2<true>(srdK, st.dk0, st.dk1, toff(2), toff(2), L_K + 16384 + lw);
  dma2<true>(srdV, st.dv0, st.dv1, toff(1), toff(1), L_V + 8192 + lw);
  bar();
  for (int v = 0; v < 15; ++v) bar();
  waitv<0>();
  bar();
  for (int i = 0; i < 4; ++i) {
    const unsigned ob = (bh0 + 4 * (i - 1)) * OBH - 2048;
    const int v0 = 16 * i;
    const __amdgpu_buffer_rsrc_t r0 = o_rsrc(ows, i > 0);
    st.tra = opq(st.qa - 2048);
    a_interval<0, 1, 1, true, true, 0, true, false>(st, dmv(v0), lw, r0, ob); waitv<10>(); bar();
    a_interval<1, 1, 2, true, false, 1, true, false>(st, dmv(v0 + 1), lw, r0, ob); waitv<9>(); bar();
    a_interval<2, 2, 1, true, false, 0, true, true>(st, dmv(v0 + 2), lw, r0, ob); waitv<9>(); bar();
    a_interval<3, 1, 1, true, false, 0, true, true>(st, dmv(v0 + 3), lw, r0, ob); waitv<10>(); bar();
    for (int g = 1; g < 4; ++g) {
      const int v = v0 + 4 * g;
      const __amdgpu_buffer_rsrc_t r1 = o_rsrc(ows, i > 0 && g < 3), r2 = o_rsrc(ows, i > 0 && g < 2);
      st.tra = opq(st.tra + 4096);
      a_interval<0, 1, 1, true, false, 0, true, true>(st, dmv(v), lw, r1, ob + g * 4096); waitv<11>(); bar();
      a_interval<1, 1, 1, true, false, 0, true, true>(st, dmv(v + 1), lw, r1, ob + g * 4096); waitv<11>(); bar();
      a_interval<2, 1, 1, true, false, 0, true, true>(st, dmv(v + 2), lw, r2, ob + g * 4096); waitv<11>(); bar();
      a_interval<3, 1, 1, true, false, 0, true, true>(st, dmv(v + 3), lw, r2, ob + g * 4096); waitv<11>(); bar();
    }
  }
  const unsigned ob = (bh0 + 12) * OBH;
  const __amdgpu_buffer_rsrc_t ro = o_rsrc(ows, true);
  a_interval<0, 1, 1, false, false, 0, false, false>(st, dmv(0), lw, ro, ob);
  a_interval<1, 1, 0, false, false, 2, false, false>(st, dmv(0), lw, ro, ob);
}

template <int NQ> struct PState {
  f32x4 acc[NQ][8];
  bf16x8 xs[2][NQ][2], xc[2][NQ], wf[10];
  unsigned wa[8], xo, xw[2], xr[2], dw, pw;
  i32x4 sw, sx;
  unsigned wb0, wb1, lw;
  f32x4 o8[8], s[2][2];
  bf16x8 qf[4], vf[8];
  u32x4 pk;
  unsigned ka[4], va[8], qa, qw;
  float mc, l0;
};
constexpr int p_nw(int iv) { return (iv & 3) == 3 ? 4 : 2; }
constexpr int p_nrest(int NQ, int iv) {
  iv &= 15;
  int n = NQ == 2 ? p_nw(iv) / 2 : 0;
  for (int it = 10 * iv; it < 10 * iv + 10; ++it) if ((it & 15) >= 3 && (it & 15) < 3 + NQ) n += 2;
  if (NQ == 1 && iv >= 2 && iv < 6) n += 1;
  return n;
}
constexpr int p_wait(int NQ, int iv) { return p_nrest(NQ, iv + 15) + p_nw(iv) + p_nrest(NQ, iv); }

template <int NQ> DI unsigned p_woff(const PState<NQ>& st, int ks) {
  return (ks < 20 ? ks : ks - 20) * (32u * NH * DH * 2) + (ks < 20 ? st.wb0 : st.wb1);
}
template <int NQ> DI void p_rdw(PState<NQ>& st, int it) {
  const int sl = (it >> 3) % 5, t = it & 7;
  st.wf[it % 10] = lds_tr2(st.wa[t] + sl * 8192, st.wa[t] + (sl * 8192 + 1024));
}
template <int NQ> DI void p_ldx(PState<NQ>& st, int D, int q) {
  ldx(st.xs[D & 1][q][0], st.sx, st.xo, (D % 10) * 128 + q * (16 * DM * 2));
  ldx(st.xs[D & 1][q][1], st.sx, st.xo, (D % 10) * 128 + q * (16 * DM * 2) + 8 * DM * 2);
}
template <int NQ> DI void p_wrx(PState<NQ>& st, int D, int q) {
  *(LDSP(bf16x8))(st.xw[0] + q * 2048) = st.xs[D & 1][q][0];
  *(LDSP(bf16x8))(st.xw[1] + q * 2048) = st.xs[D & 1][q][1];
}
template <int NQ> DI void p_item(PState<NQ>& st, int it) {
  const int ks = it >> 3, t = it & 7, D = it >> 4, u = it & 15;
#pragma unroll
  for (int q = 0; q < NQ; ++q) {
    if (ks == 0) { f32x4 z = {}; st.acc[q][t] = MFMA16(st.wf[it % 10], st.xc[0][q], z); }
    else st.acc[q][t] = MFMA16(st.wf[it % 10], st.xc[ks & 1][q], st.acc[q][t]);
  }
  if (it % 10 == 0) {
    const int iv = it / 10, k1 = 5 * (iv >> 2) + 4 + (iv & 3);
#pragma unroll
    for (int k = k1; k < k1 + p_nw(iv) / 2; ++k)
      dma2<false>(st.sw, st.dw, st.dw, p_woff(st, k), p_woff(st, k) + 4 * NH * DH * 2 - 1024, L_W + (k % 5) * 8192 + st.lw);
    if constexpr (NQ == 2) {
#pragma unroll
      for (int k = k1; k < k1 + p_nw(iv) / 2; ++k) dma1<false, 0>(st.sw, st.pw, p_woff(st, k + 8), L_D);
    }
  }
  if (u == 0 || u == 8) {
#pragma unroll
    for (int q = 0; q < NQ; ++q) st.xc[u == 0][q] = lds_b128(st.xr[u == 0] + q * 2048);
  }
  if (u >= 2 && u < 2 + NQ) p_wrx(st, D + 1, u - 2);
  if (u >= 3 && u < 3 + NQ) p_ldx(st, D + 3, u - 3);
}
template <int NQ> DI void p_handq(PState<NQ>& st, unsigned qb0, int lane) {
  const unsigned c16 = lane & 15, g = lane >> 4;
#pragma unroll
  for (int q = 0; q < NQ; ++q) {
    const unsigned qb = qb0 + q;
    const unsigned qw = NQ == 1 ? st.qw : opq(L_Q + ((qb >> 1) * 512 + (g >> 1) * 32 + (qb & 1) * 16 + c16) * 16 + (g & 1) * 8);
#pragma unroll
    for (int t = 0; t < 8; ++t) {
      u32x2 w = {pk2(st.acc[q][t][0], st.acc[q][t][1]), pk2(st.acc[q][t][2], st.acc[q][t][3])};
      *(LDSP(u32x2))(qw + (NQ == 1 ? t * 512 : t * 1024)) = w;
    }
  }
}

template <int IV, bool PR, int PV, int SM, bool QK, bool LQ, int EPI, bool TR>
DI void p_att(PState<1>& st, __amdgpu_buffer_rsrc_t ro, unsigned obase) {
  constexpr int SL = IV & 3, X = (SL + 1) & 1, Y = SL & 1, I0 = 10 * IV;
  bf16x8 kf[8], ot;
  float mx = 0.f;
  const float nm = -st.mc;
  const bf16x8 pf = __builtin_bit_cast(bf16x8, st.pk);
  auto pv = [&](int t) {
    if constexpr (PV == 2) { f32x4 z = {}; st.o8[t] = MFMA16(st.vf[t], pf, z); }
    else if constexpr (PV == 1) st.o8[t] = MFMA16(st.vf[t], pf, st.o8[t]);
  };
#pragma unroll
  for (int i = 0; i < 7; ++i) {
    if (i < 3) pv(i); else pv(i + 1);
    if constexpr (PR) p_rdw(st, I0 + 2 + i);
    if constexpr (QK) if (i < 4) {
      kf[2 * i] = lds_b128(st.ka[i] + (L_K + SL * 8192));
      kf[2 * i + 1] = lds_b128(st.ka[i] + (L_K + SL * 8192 + 4096));
    }
    if constexpr (LQ) if (i < 4) st.qf[i] = lds_b128(st.qa + i * 1024);
    if constexpr (TR) if (i == 6) ot = lds_b128(st.qa + (IV - 2) * 1024);
    if (i == 2) pv(3);
    else if constexpr (PR) p_item(st, I0 + (i < 2 ? i : i - 1));
    if (i >= 1 && i < 5) {
      const int r = i - 1;
      if constexpr (SM == 1) {
        st.s[X][0][r] = vfma(st.s[X][0][r], CSC, nm);
        st.s[X][1][r] = vfma(st.s[X][1][r], CSC, nm);
        mx = r == 0 ? vmax(st.s[X][0][0], st.s[X][1][0]) : vmax3(mx, st.s[X][0][r], st.s[X][1][r]);
      } else if constexpr (SM == 2) {
        mx = r == 0 ? fmaxf(st.s[X][0][0], st.s[X][1][0]) : fmaxf(fmaxf(mx, st.s[X][0][r]), st.s[X][1][r]);
      }
      if constexpr (PV != 0) st.l0 = vadd(st.l0, vadd(st.s[Y][0][r], st.s[Y][1][r]));
    }
    SB();
  }
  float inv = 0.f;
  if constexpr (EPI != 0) inv = __builtin_amdgcn_rcpf(swap32add(swap16add(st.l0)));
  if constexpr (SM == 2) {
    st.mc = swap32max(swap16max(mx)) * CSC;
    st.l0 = 0.f;
    const float n2 = -st.mc;
#pragma unroll
    for (int r = 0; r < 4; ++r) { st.s[X][0][r] = vfma(st.s[X][0][r], CSC, n2); st.s[X][1][r] = vfma(st.s[X][1][r], CSC, n2); }
  } else if constexpr (SM == 1) {
    if (__builtin_expect(__any(mx > THR2), 0)) {
      const float dl = fmaxf(swap32max(swap16max(mx)), 0.f);
      const float f = __builtin_amdgcn_exp2f(-dl);
      st.s[X][0] -= dl; st.s[X][1] -= dl;
#pragma unroll
      for (int t = 0; t < 8; ++t) st.o8[t] *= f;
      st.l0 *= f; st.mc += dl;
    }
  }
  SB();
  constexpr unsigned VB = L_V + ((SL + 3) & 3) * 8192;
#pragma unroll
  for (int i = 0; i < 4; ++i) {
    if constexpr (QK) {
      if (i == 0) { f32x4 z = {}; st.s[Y][0] = MFMA16(kf[0], st.qf[0], z); st.s[Y][1] = MFMA16(kf[1], st.qf[0], z); }
      else { st.s[Y][0] = MFMA16(kf[2 * i], st.qf[i], st.s[Y][0]); st.s[Y][1] = MFMA16(kf[2 * i + 1], st.qf[i], st.s[Y][1]); }
    }
    if constexpr (PR) if (i == 0) p_rdw(st, I0 + 9);
    if constexpr (SM != 0) {
      st.vf[2 * i] = lds_tr2(st.va[2 * i] + VB, st.va[2 * i] + (VB + 4096));
      st.vf[2 * i + 1] = lds_tr2(st.va[2 * i + 1] + VB, st.va[2 * i + 1] + (VB + 4096));
      const int u = i >> 1, r = (i & 1) * 2;
      st.s[X][u][r] = __builtin_amdgcn_exp2f(st.s[X][u][r]);
      st.s[X][u][r + 1] = __builtin_amdgcn_exp2f(st.s[X][u][r + 1]);
      if (i > 0) st.pk[i - 1] = pk2(st.s[X][(i - 1) >> 1][((i - 1) & 1) * 2], st.s[X][(i - 1) >> 1][((i - 1) & 1) * 2 + 1]);
    }
    if constexpr (PR) p_item(st, I0 + 6 + i);
    if constexpr (TR) if (i == 3)
      __builtin_amdgcn_raw_buffer_store_b128(__builtin_bit_cast(u32x4, ot), ro, st.qa - L_Q, obase + (IV - 2) * 1024, O_AUX);
    if constexpr (EPI != 0) {
#pragma unroll
      for (int t = 2 * i; t < 2 * i + 2; ++t) {
        u32x2 w = {pk2(vmul(st.o8[t][0], inv), vmul(st.o8[t][1], inv)), pk2(vmul(st.o8[t][2], inv), vmul(st.o8[t][3], inv))};
        if constexpr (EPI == 1) *(LDSP(u32x2))(st.qw + t * 512) = w;
        else __builtin_amdgcn_raw_buffer_store_b64(w, ro, st.qw - L_Q, obase + t * 512, O_AUX);
      }
    }
    SB();
  }
  if constexpr (PR) { p_rdw(st, I0 + 10); p_rdw(st, I0 + 11); }
  if constexpr (SM != 0) st.pk[3] = pk2(st.s[X][1][2], st.s[X][1][3]);
}

template <int NQ>
DI void p_role(int p, int qb0, int lane, i32x4 srdX, i32x4 srdW, unsigned b, unsigned h0, unsigned j, char* ows) {
  constexpr bool ATT = NQ == 1;
  PState<NQ> st;
  const unsigned c16 = lane & 15, g = lane >> 4, qq = (lane >> 2) & 3, pp = lane & 3;
#pragma unroll
  for (int t = 0; t < 8; ++t)
    st.wa[t] = opq(L_W + (8 * g + qq) * 256 + ((((2 * t) ^ (qq << 2) ^ ((g & 1) << 1)) | (pp >> 1)) << 4) + (pp & 1) * 8);
  st.xo = (b * SQ + 16 * qb0 + (lane >> 3)) * (DM * 2) + (lane & 7) * 16;
  st.xw[0] = opq(L_X + qb0 * 2048 + (lane >> 3) * 128 + (((lane & 7) ^ (lane >> 4)) << 4));
  st.xw[1] = opq(L_X + qb0 * 2048 + 1024 + (lane >> 3) * 128 + (((lane & 7) ^ (lane >> 4) ^ 4) << 4));
  st.xr[0] = opq(L_X + qb0 * 2048 + c16 * 128 + ((g ^ (c16 >> 1)) << 4));
  st.xr[1] = opq(st.xr[0] ^ 64);
  st.dw = (8 * p + g) * (NH * DH * 2) + ((c16 ^ (g << 2) ^ ((p & 1) << 1)) << 4);
  st.pw = ((j >> 2) * 4 + g) * (NH * DH * 2) + c16 * 16;
  st.sw = srdW; st.sx = srdX; st.lw = p * 2048;
  st.wb0 = h0 * 256; st.wb1 = (h0 + 4) * 256;
  if constexpr (ATT) {
#pragma unroll
    for (int k = 0; k < 4; ++k) st.ka[k] = opq(c16 * 256 + (((4 * k + g) ^ c16) << 4));
#pragma unroll
    for (int t = 0; t < 8; ++t) st.va[t] = opq(st.wa[t] - L_W - g * 1024);
    st.qa = opq(L_Q + 32768 + lane * 16);
    st.qw = opq(L_Q + 32768 + ((g >> 1) * 16 + c16) * 16 + (g & 1) * 8);
    st.mc = 0.f; st.l0 = 0.f; st.pk = u32x4{};
    st.s[0][0] = f32x4{}; st.s[0][1] = f32x4{}; st.s[1][0] = f32x4{}; st.s[1][1] = f32x4{};
#pragma unroll
    for (int t = 0; t < 8; ++t) { st.vf[t] = bf16x8{}; st.o8[t] = f32x4{}; }
  }
  const unsigned bh0 = b * NH + h0;
#pragma unroll
  for (int q = 0; q < NQ; ++q) {
    st.xs[0][q][0] = bf16x8{}; st.xs[0][q][1] = bf16x8{}; st.xs[1][q][0] = bf16x8{}; st.xs[1][q][1] = bf16x8{};
    p_ldx(st, 0, q);
  }
#pragma unroll
  for (int k = 0; k < 4; ++k) dma2<false>(srdW, st.dw, st.dw, p_woff(st, k), p_woff(st, k) + 4 * NH * DH * 2 - 1024, L_W + k * 8192 + st.lw);
#pragma unroll
  for (int q = 0; q < NQ; ++q) p_ldx(st, 1, q);
  waitv<0>(); SB();
#pragma unroll
  for (int q = 0; q < NQ; ++q) { p_wrx(st, 0, q); st.xc[0][q] = lds_b128(st.xr[0] + q * 2048); }
  SB();
#pragma unroll
  for (int q = 0; q < NQ; ++q) p_ldx(st, 2, q);
  if constexpr (NQ == 2) { dma1<false, 0>(st.sw, st.pw, p_woff(st, 10), L_D); dma1<false, 0>(st.sw, st.pw, p_woff(st, 11), L_D); }
  bar();
  p_rdw(st, 0); p_rdw(st, 1);
  for (int i = -1; i < 3; ++i) {
    const unsigned ob = (bh0 + 4 * (i - 1)) * OBH;
    const __amdgpu_buffer_rsrc_t ro = o_rsrc(ows, i > 0);
    sfor<16>([&](auto IVc) {
      constexpr int IV = decltype(IVc)::value;
      if constexpr (ATT) {
        p_att<IV, true, IV == 2 ? 2 : 1, IV == 1 ? 2 : 1, true, IV == 0, IV == 1 ? 1 : 0, (IV >= 2 && IV < 6)>(st, ro, ob);
      } else {
#pragma unroll
        for (int it = 10 * IV; it < 10 * IV + 10; ++it) { p_rdw(st, it + 2); p_item(st, it); SB(); }
      }
      if constexpr (IV == 15) p_handq(st, qb0, lane);
      waitv<p_wait(NQ, IV)>(); bar();
    });
    st.wb0 = st.wb1; st.wb1 = (h0 + 4 * ((i + 3) & 3)) * 256;
  }
  waitv<0>(); SB();
  if constexpr (!ATT) {
    for (int v = 0; v < 16; ++v) bar();
  } else {
    unsigned ob = (bh0 + 8) * OBH;
    const __amdgpu_buffer_rsrc_t ro = o_rsrc(ows, true);
    sfor<8>([&](auto IVc) {
      constexpr int IV = decltype(IVc)::value;
      p_att<IV, false, IV == 2 ? 2 : 1, IV == 1 ? 2 : 1, true, IV == 0, IV == 1 ? 1 : 0, (IV >= 2 && IV < 6)>(st, ro, ob);
      bar();
    });
    for (int gq = 2; gq < 4; ++gq) {
      p_att<8, false, 1, 1, true, false, 0, false>(st, ro, ob); bar();
      p_att<9, false, 1, 1, true, false, 0, false>(st, ro, ob); bar();
      p_att<10, false, 1, 1, true, false, 0, false>(st, ro, ob); bar();
      p_att<11, false, 1, 1, true, false, 0, false>(st, ro, ob); bar();
    }
    ob = (bh0 + 12) * OBH;
    p_att<0, false, 1, 1, false, false, 0, false>(st, ro, ob);
    p_att<1, false, 1, 0, false, false, 2, false>(st, ro, ob);
  }
}

__global__ __launch_bounds__(512, 2) void attn_fused(const char* __restrict__ x, const char* __restrict__ kc, const char* __restrict__ vc,
                                                     const char* __restrict__ wq, char* __restrict__ ows) {
  const int wid = __builtin_amdgcn_readfirstlane(threadIdx.x >> 6), lane = threadIdx.x & 63;
  const unsigned w = blockIdx.x, xcd = w & 7, j = w >> 3, b = 8 * (xcd >> 1) + (j >> 2), h0 = 16 * (xcd & 1) + (j & 3);
  const i32x4 srdW = mk_srd(wq, DM * NH * DH * 2), srdX = mk_srd(x, NB * SQ * DM * 2);
  if (wid < 4) {
    a_role(wid, lane, mk_srd(kc, NB * NH * SK * DH * 2), mk_srd(vc, NB * NH * SK * DH * 2), srdX, srdW, b, h0, j, ows);
  } else {
    if (wid == 4) p_role<1>(0, 8, lane, srdX, srdW, b, h0, j, ows);
    else if (wid == 7) p_role<2>(3, 6, lane, srdX, srdW, b, h0, j, ows);
    else p_role<3>(wid - 4, 3 * (wid - 5), lane, srdX, srdW, b, h0, j, ows);
  }
}

constexpr int K2_ST = 4;
constexpr unsigned K2_LDS = 147456;

__global__ __launch_bounds__(512, 2) void out_proj(const char* __restrict__ ows, const char* __restrict__ wo, char* __restrict__ out) {
  const int wid = __builtin_amdgcn_readfirstlane(threadIdx.x >> 6), lane = threadIdx.x & 63;
  const unsigned xcd = blockIdx.x & 7, j = blockIdx.x >> 3, rt = 6 * xcd + j / 5, ct = j % 5;
  const unsigned qg = wid & 1, kg = wid >> 1;
  const unsigned c16 = lane & 15, g = lane >> 4, qq = (lane >> 2) & 3, pp = lane & 3;
  unsigned wa[8], xo[3];
#pragma unroll
  for (int t = 0; t < 8; ++t)
    wa[t] = kg * 8192 + (8 * g + qq) * 256 + ((((2 * t) ^ (qq << 2) ^ ((g & 1) << 1)) | (pp >> 1)) << 4) + (pp & 1) * 8;
#pragma unroll
  for (int q = 0; q < 3; ++q) {
    const unsigned Qb = 6 * rt + 3 * qg + q, bb = Qb / 9, qi = Qb % 9;
    xo[q] = bb * (NH * OBH) + qi * 4096 + kg * 1024 + lane * 16;
  }
  const i32x4 srdW = mk_srd(wo, NH * DH * DM * 2);
  const i32x4 ro = mk_srd(ows, NB * SQ * NH * DH * 2);
  const unsigned dw0 = (16 * wid + g) * (DM * 2) + ct * 256 + ((c16 ^ (g << 2)) << 4), dw1 = dw0 ^ 32;
  auto wissue = [&](int s, int slot) {
    const unsigned so = (unsigned)(s & 31) * (128u * DM * 2);
    dma2<false>(srdW, dw0, dw0, so, so + 4 * DM * 2 - 1024, slot * 32768 + wid * 4096);
    dma2<false>(srdW, dw1, dw1, so + 8 * DM * 2, so + 12 * DM * 2 - 1024, slot * 32768 + wid * 4096 + 2048);
  };
  f32x4 acc[3][8];
#pragma unroll
  for (int q = 0; q < 3; ++q)
#pragma unroll
    for (int t = 0; t < 8; ++t) acc[q][t] = f32x4{};
  bf16x8 xf[K2_ST][3] = {};
  auto xload = [&](bf16x8 (&dst)[3], int s) {
#pragma unroll
    for (int q = 0; q < 3; ++q)
      ldx(dst[q], ro, xo[q], (unsigned)(s & 31) * OBH);
  };
#pragma unroll
  for (int s = 0; s < K2_ST - 1; ++s) { wissue(s, s); xload(xf[s], s); }
  waitv<7 * (K2_ST - 2)>();
  bar();
  for (int s0 = 0; s0 < 32; s0 += K2_ST) {
#pragma unroll
    for (int u = 0; u < K2_ST; ++u) {
      wissue(s0 + u + K2_ST - 1, (u + K2_ST - 1) % K2_ST);
      xload(xf[(u + K2_ST - 1) % K2_ST], s0 + u + K2_ST - 1);
#pragma unroll
      for (int t = 0; t < 8; ++t) {
        const bf16x8 wf = lds_tr2(wa[t] + u * 32768, wa[t] + u * 32768 + 1024);
#pragma unroll
        for (int q = 0; q < 3; ++q) acc[q][t] = MFMA16(wf, xf[u][q], acc[q][t]);
      }
      waitv<7 * (K2_ST - 2)>();
      bar();
    }
  }
  waitv<0>();
  bar();
#pragma unroll
  for (int t = 0; t < 8; ++t) {
    const unsigned dk = t >> 1;
    if (dk != kg) {
      const unsigned si = kg - (kg > dk ? 1 : 0);
#pragma unroll
      for (int q = 0; q < 3; ++q) *(LDSP(f32x4))(((((dk * 3 + si) * 2 + qg) * 3 + q) * 2 + (t & 1)) * 1024 + lane * 16) = acc[q][t];
    }
  }
  bar();
#pragma unroll
  for (int q = 0; q < 3; ++q) {
#pragma unroll
    for (int tt = 0; tt < 2; ++tt) {
      f32x4 v = kg == 0 ? acc[q][tt] : kg == 1 ? acc[q][2 + tt] : kg == 2 ? acc[q][4 + tt] : acc[q][6 + tt];
#pragma unroll
      for (int si = 0; si < 3; ++si) v += *(LDSP(f32x4))(((((kg * 3 + si) * 2 + qg) * 3 + q) * 2 + tt) * 1024 + lane * 16);
      u32x2 w = {pk2(v[0], v[1]), pk2(v[2], v[3])};
      const unsigned row = 96 * rt + 48 * qg + 16 * q + c16, col = 128 * ct + 32 * kg + 16 * tt + 4 * g;
      *(u32x2*)(out + (row * DM + col) * 2) = w;
    }
  }
}

extern "C" void kernel_launch(void* const* d_in, const int* in_sizes, int n_in, void* d_out, int out_size, void* d_ws, size_t ws_size,
                              hipStream_t stream) {
  (void)in_sizes; (void)n_in; (void)out_size; (void)ws_size;
  (void)hipFuncSetAttribute((const void*)attn_fused, hipFuncAttributeMaxDynamicSharedMemorySize, (int)L_END);
  (void)hipFuncSetAttribute((const void*)out_proj, hipFuncAttributeMaxDynamicSharedMemorySize, (int)K2_LDS);
  attn_fused<<<256, 512, L_END, stream>>>((const char*)d_in[0], (const char*)d_in[1], (const char*)d_in[2], (const char*)d_in[3], (char*)d_ws);
  out_proj<<<240, 512, K2_LDS, stream>>>((const char*)d_ws, (const char*)d_in[4], (char*)d_out);
}
```

```cpp
#include <hip/hip_runtime.h>
#include <hip/hip_bf16.h>
#include <type_traits>

typedef __bf16 bf16x8 __attribute__((ext_vector_type(8)));
typedef __bf16 bf16x4 __attribute__((ext_vector_type(4)));
typedef __bf16 bf16x2 __attribute__((ext_vector_type(2)));
typedef float f32x16 __attribute__((ext_vector_type(16)));
typedef float f32x4 __attribute__((ext_vector_type(4)));
typedef float f32x2 __attribute__((ext_vector_type(2)));
typedef unsigned u32x4 __attribute__((ext_vector_type(4)));
typedef unsigned u32x2 __attribute__((ext_vector_type(2)));
typedef int i32x4 __attribute__((ext_vector_type(4)));
#define LDSP(T) __attribute__((address_space(3))) T*
#define DI __device__ __forceinline__

constexpr int NB = 32, SQ = 144, DM = 640, NH = 32, SK = 512, DH = 128;
constexpr float CSC = 0.088388347648318440f * 1.4426950408889634f;
constexpr float THR2 = 8.0f;

#define O_AUX 16
constexpr unsigned L_K = 0, L_V = 32768, L_W = 65536, L_Q = 106496, L_X = 143360, L_D = 161792, L_END = 162816;
constexpr unsigned X_OFF = 40u << 20, F_OFF = 56u << 20;
constexpr unsigned OBH = SQ * DH * 2;

DI unsigned pk2(float a, float b) {
  f32x2 v = {a, b};
  return __builtin_bit_cast(unsigned, __builtin_convertvector(v, bf16x2));
}
DI bf16x8 pk8(float a, float b, float c, float d, float e, float f, float g, float h) {
  u32x4 w = {pk2(a, b), pk2(c, d), pk2(e, f), pk2(g, h)};
  return __builtin_bit_cast(bf16x8, w);
}
DI bf16x8 lds_b128(unsigned a) { return *(LDSP(bf16x8))(a); }
DI bf16x8 lds_tr2(unsigned a0, unsigned a1) {
  bf16x4 lo = __builtin_amdgcn_ds_read_tr16_b64_v4bf16((LDSP(bf16x4))(a0));
  bf16x4 hi = __builtin_amdgcn_ds_read_tr16_b64_v4bf16((LDSP(bf16x4))(a1));
  return __builtin_shufflevector(lo, hi, 0, 1, 2, 3, 4, 5, 6, 7);
}
DI i32x4 mk_srd(const void* p, unsigned bytes) {
  unsigned long long a = (unsigned long long)p;
  i32x4 r = {(int)(unsigned)a, (int)(unsigned)(a >> 32), (int)bytes, 0x00020000};
  return r;
}
template <bool NT>
DI void dma2(i32x4 srd, unsigned v0, unsigned v1, unsigned s0, unsigned s1, unsigned ldsaddr) {
  if constexpr (NT)
    asm volatile("s_mov_b32 m0, %5\n\ts_nop 0\n\tbuffer_load_dwordx4 %0, %2, %3 offen nt lds\n\t"
                 "buffer_load_dwordx4 %1, %2, %4 offen offset:1024 nt lds"
                 :: "v"(v0), "v"(v1), "s"(srd), "s"(s0), "s"(s1), "s"(ldsaddr) : "memory");
  else
    asm volatile("s_mov_b32 m0, %5\n\ts_nop 0\n\tbuffer_load_dwordx4 %0, %2, %3 offen lds\n\t"
                 "buffer_load_dwordx4 %1, %2, %4 offen offset:1024 lds"
                 :: "v"(v0), "v"(v1), "s"(srd), "s"(s0), "s"(s1), "s"(ldsaddr) : "memory");
}
template <bool NT, int OFF>
DI void dma1(i32x4 srd, unsigned v0, unsigned s0, unsigned ldsaddr) {
  if constexpr (NT)
    asm volatile("s_mov_b32 m0, %3\n\ts_nop 0\n\tbuffer_load_dwordx4 %0, %1, %2 offen offset:%4 nt lds"
                 :: "v"(v0), "s"(srd), "s"(s0), "s"(ldsaddr), "n"(OFF) : "memory");
  else
    asm volatile("s_mov_b32 m0, %3\n\ts_nop 0\n\tbuffer_load_dwordx4 %0, %1, %2 offen offset:%4 lds"
                 :: "v"(v0), "s"(srd), "s"(s0), "s"(ldsaddr), "n"(OFF) : "memory");
}
template <bool NT, int OFF, bool M0> DI void dmak(i32x4 srd, unsigned v0, unsigned s0, unsigned ldsaddr) {
  if constexpr (M0) dma1<NT, OFF>(srd, v0, s0, ldsaddr);
  else if constexpr (NT) asm volatile("buffer_load_dwordx4 %0, %1, %2 offen offset:%3 nt lds" :: "v"(v0), "s"(srd), "s"(s0), "n"(OFF) : "memory");
  else asm volatile("buffer_load_dwordx4 %0, %1, %2 offen offset:%3 lds" :: "v"(v0), "s"(srd), "s"(s0), "n"(OFF) : "memory");
}
DI void ldx(bf16x8& d, i32x4 srd, unsigned v0, unsigned s0) {
  asm volatile("buffer_load_dwordx4 %0, %1, %2, %3 offen" : "+v"(d) : "v"(v0), "s"(srd), "s"(s0) : "memory");
}
DI __amdgpu_buffer_rsrc_t o_rsrc(char* ows, bool live) {
  return __builtin_amdgcn_make_buffer_rsrc((void*)ows, 0, live ? NB * SQ * NH * DH * 2 : 0, 0x00020000);
}
template <int AUX = O_AUX> DI void st128(bf16x8 d, __amdgpu_buffer_rsrc_t r, unsigned v, unsigned so) {
  __builtin_amdgcn_raw_buffer_store_b128(__builtin_bit_cast(u32x4, d), r, v, so, AUX);
  asm volatile("s_nop 1" :: "v"(d) : "memory");
}
template <int N, class F> DI void sfor(F&& f) {
  if constexpr (N > 0) { sfor<N - 1>(f); f(std::integral_constant<int, N - 1>{}); }
}
#define SB() __builtin_amdgcn_sched_barrier(0)
DI float vfma(float a, float b, float c) { float r = __builtin_fmaf(a, b, c); asm("" : "+v"(r)); return r; }
DI float vadd(float a, float b) { float r = a + b; asm("" : "+v"(r)); return r; }
DI float vmul(float a, float b) { float r = a * b; asm("" : "+v"(r)); return r; }
DI float vmax(float a, float b) { return fmaxf(a, b); }
DI float vmax3(float a, float b, float c) { return fmaxf(fmaxf(a, b), c); }
DI unsigned opq(unsigned v) { asm volatile("" : "+v"(v)); return v; }
template <int N> DI void waitv() { asm volatile("s_waitcnt vmcnt(%0)" :: "n"(N) : "memory"); }
template <int LG = 0> DI void bar() {
  asm volatile("" ::: "memory");
  __builtin_amdgcn_s_waitcnt(0xC07F | (LG << 8));
  __builtin_amdgcn_s_barrier();
  asm volatile("" ::: "memory");
}
DI float swap32max(float v) {
  auto r = __builtin_amdgcn_permlane32_swap(__float_as_uint(v), __float_as_uint(v), false, false);
  return fmaxf(__uint_as_float(r[0]), __uint_as_float(r[1]));
}
DI float swap32add(float v) {
  auto r = __builtin_amdgcn_permlane32_swap(__float_as_uint(v), __float_as_uint(v), false, false);
  return __uint_as_float(r[0]) + __uint_as_float(r[1]);
}
DI float swap16max(float v) {
  auto r = __builtin_amdgcn_permlane16_swap(__float_as_uint(v), __float_as_uint(v), false, false);
  return fmaxf(__uint_as_float(r[0]), __uint_as_float(r[1]));
}
DI float swap16add(float v) {
  auto r = __builtin_amdgcn_permlane16_swap(__float_as_uint(v), __float_as_uint(v), false, false);
  return __uint_as_float(r[0]) + __uint_as_float(r[1]);
}
#define MFMA32(a, b, c) __builtin_amdgcn_mfma_f32_32x32x16_bf16(a, b, c, 0, 0, 0)
#define MFMA16(a, b, c) __builtin_amdgcn_mfma_f32_16x16x32_bf16(a, b, c, 0, 0, 0)

constexpr unsigned WTILE = 32u * NH * DH * 2;
struct AState {
  f32x16 o[4];
  f32x16 s[2], nm;
  bf16x8 qf[8], vf[8];
  u32x4 pk[2];
  float l0, l1;
  unsigned ka[8], va[4], qa;
  unsigned ost, tra, trg, dw, lw;
};
DI void touch(i32x4 srd, unsigned v0, unsigned s0) {
  asm volatile("s_mov_b32 m0, %3\n\ts_nop 0\n\tbuffer_load_dword %0, %1, %2 offen lds" :: "v"(v0), "s"(srd), "s"(s0), "s"(L_D) : "memory");
}
DI f32x16 splat16(float v) { f32x16 r = {v, v, v, v, v, v, v, v, v, v, v, v, v, v, v, v}; asm("" : "+v"(r)); return r; }

template <int SL, int PV, int SM, bool QK, bool LQ, int EPI, bool TR, bool WD>
DI void a_interval(AState& st, __amdgpu_buffer_rsrc_t ro, unsigned obase, i32x4 sw, unsigned ow) {
  constexpr int X = (SL + 1) & 1, Y = SL & 1;
  constexpr unsigned VB = L_V + ((SL + 3) & 3) * 8192;
  bf16x8 kf[8], ot;
  float mx = 0.f, f = 1.f, inv = 0.f;
  bool resc = false;
  f32x16& s = st.s[X];
  auto E = [&](int j) { s[2 * j] = __builtin_amdgcn_exp2f(s[2 * j]); s[2 * j + 1] = __builtin_amdgcn_exp2f(s[2 * j + 1]); };
  auto A = [&](int j) { st.l0 = vadd(st.l0, s[2 * j]); st.l1 = vadd(st.l1, s[2 * j + 1]); };
  auto C = [&](int j) { st.pk[j >> 2][j & 3] = pk2(s[2 * j], s[2 * j + 1]); };
  auto V = [&](int n) {
    st.vf[n] = lds_tr2(st.va[n & 3] + (VB + (n >> 2) * 4096), st.va[n & 3] + (VB + (n >> 2) * 4096 + 2048));
  };
#pragma unroll
  for (int g = 0; g < 16; ++g) {
    if (g < 8) {
      if constexpr (PV != 0) {
        const bf16x8 pf = __builtin_bit_cast(bf16x8, st.pk[g >> 2]);
        if (PV == 2 && g < 4) { f32x16 z = {}; st.o[g & 3] = MFMA32(st.vf[g], pf, z); }
        else st.o[g & 3] = MFMA32(st.vf[g], pf, st.o[g & 3]);
      }
    } else if constexpr (QK) {
      if (g == 8) {
        __builtin_amdgcn_s_waitcnt(LQ ? 0xC07F : 0xC87F);
        if constexpr (LQ) { f32x16 z = {}; st.s[Y] = MFMA32(kf[0], st.qf[0], z); }
        else st.s[Y] = MFMA32(kf[0], st.qf[0], st.nm);
      } else st.s[Y] = MFMA32(kf[g - 8], st.qf[g - 8], st.s[Y]);
    }
    if constexpr (WD) if (g == 0) {
      dma2<false>(sw, st.dw, st.dw, ow, ow + 4 * NH * DH * 2 - 1024, L_W + ((4 + SL) % 5) * 8192 + st.lw);
      if constexpr (SL == 3) dma2<false>(sw, st.dw, st.dw, ow + WTILE, ow + WTILE + 4 * NH * DH * 2 - 1024, L_W + 3 * 8192 + st.lw);
    }
    if constexpr (QK) if (g < 3) {
#pragma unroll
      for (int k = 3 * g; k < 3 * g + 3 && k < 8; ++k) kf[k] = lds_b128(st.ka[k] + (L_K + SL * 8192));
    }
    if constexpr (TR) if (g == 2) ot = lds_b128(st.tra + SL * 1024);
    if constexpr (LQ) if (g >= 2 && g < 6) { st.qf[2 * g - 4] = lds_b128(st.qa + (2 * g - 4) * 1024); st.qf[2 * g - 3] = lds_b128(st.qa + (2 * g - 3) * 1024); }
    if constexpr (SM != 0) {
      if (g == 0) { mx = vmax3(s[0], s[1], s[2]); mx = vmax3(mx, s[3], s[4]); mx = vmax3(mx, s[5], s[6]); }
      if (g == 1) { mx = vmax3(mx, s[7], s[8]); mx = vmax3(mx, s[9], s[10]); mx = vmax3(mx, s[11], s[12]); }
      if (g == 2) { mx = vmax3(mx, s[13], s[14]); mx = vmax3(mx, s[15], s[15]); }
      if (g == 3) { E(0); V(0); }
      if (g == 4) { A(0); C(0); V(1); }
      if (g == 5) { E(1); V(2); }
      if (g == 6) { E(2); A(1); }
      if (g == 7) { C(1); A(2); C(2); V(3); }
      if (g == 8) { E(3); V(4); }
      if (g == 9) { E(4); A(3); }
      if (g == 10) { C(3); A(4); C(4); V(5); }
      if (g == 11) { E(5); V(6); }
      if (g == 12) { E(6); V(7); }
      if (g == 13) { A(5); C(5); A(6); C(6); }
      if (g == 14) E(7);
      if (g == 15) { A(7); C(7); }
    }
    if constexpr (TR) if (g == 14) st128(ot, ro, st.trg, obase + SL * 1024);
    if constexpr (EPI != 0) if (g >= 8) {
      const int d = (g - 8) >> 1, r = (g & 1) * 8;
      const f32x16 o = st.o[d];
      const bf16x8 c = pk8(vmul(o[r], inv), vmul(o[r + 1], inv), vmul(o[r + 2], inv), vmul(o[r + 3], inv),
                           vmul(o[r + 4], inv), vmul(o[r + 5], inv), vmul(o[r + 6], inv), vmul(o[r + 7], inv));
      if constexpr (EPI == 1) *(LDSP(bf16x8))(st.ost + (d * 1024 + (g & 1) * 256)) = c;
      else st128(c, ro, st.ost - L_Q, obase + d * 1024 + (g & 1) * 256);
    }
    SB();
    if (g == 2) {
      if constexpr (EPI != 0) inv = __builtin_amdgcn_rcpf(swap32add(st.l0 + st.l1));
      if constexpr (SM == 2) {
        const float m = swap32max(mx);
        st.nm = splat16(-m);
        st.l0 = 0.f; st.l1 = 0.f;
#pragma unroll
        for (int r = 0; r < 16; ++r) s[r] = vadd(s[r], -m);
      } else if constexpr (SM == 1) {
        resc = __any(mx > THR2);
        if (__builtin_expect(resc, 0)) {
          const float dl = fmaxf(swap32max(mx), 0.f);
          f = __builtin_amdgcn_exp2f(-dl);
          s -= dl; st.nm -= dl;
          st.l0 *= f; st.l1 *= f;
        }
      }
      SB();
    }
    if (g == 7) {
      if constexpr (SM == 1) {
        if (__builtin_expect(resc, 0)) {
#pragma unroll
          for (int d = 0; d < 4; ++d) st.o[d] *= f;
        }
        SB();
      }
    }
  }
}

DI unsigned pf_lane(int lane, unsigned j, unsigned part) {
  const unsigned l = min(lane, 19), r = 5 * part + (l >> 2);
  return ((r >> 2) * 32 + (j >> 2) * 4 + (r & 3)) * (NH * DH * 2) + (l & 3) * 64;
}
DI void a_role(int w, int lane, i32x4 srdX, i32x4 srdW, unsigned b, unsigned h0, unsigned j, char* ows) {
  AState st;
  const unsigned qi = lane & 31, h5 = lane >> 5, bh0 = b * NH + h0;
  st.dw = (8 * w + (lane >> 4)) * (NH * DH * 2) + (((lane & 15) ^ ((lane >> 4) << 2) ^ ((w & 1) << 1)) << 4);
  st.lw = w * 2048;
  auto wdma = [&](unsigned ow, int slot) { dma2<false>(srdW, st.dw, st.dw, ow, ow + 4 * NH * DH * 2 - 1024, L_W + slot * 8192 + st.lw); };
#pragma unroll
  for (int k = 0; k < 4; ++k) wdma(k * WTILE + h0 * 256, k);
  {
    const unsigned xl = (b * SQ + (lane >> 3)) * (DM * 2) + (lane & 7) * 16, pw = pf_lane(lane, j, w);
#pragma unroll
    for (int k = 0; k < 8; ++k) {
      const unsigned id = min(16 * k + 4 * (j & 3) + w, 125u);
      dma1<false, 0>(srdX, xl, (id % 18) * (8 * DM * 2) + (3 + id / 18) * 128, L_D);
      if (k == 0 || k == 2 || k == 4) touch(srdW, pw, (5 + 5 * (k >> 1)) * WTILE + h0 * 256);
    }
  }
#pragma unroll
  for (int k = 0; k < 8; ++k) st.ka[k] = opq(qi * 256 + (((2 * k + h5) ^ (qi & 15)) << 4));
  {
    const unsigned G = lane >> 4, qq = (lane >> 2) & 3, p = lane & 3;
#pragma unroll
    for (int d = 0; d < 4; ++d)
      st.va[d] = opq((4 * h5 + qq) * 256 + ((((4 * d) ^ (qq << 2)) | ((2 * (p & 1)) ^ (h5 << 1)) | (G & 1)) << 4) + (p >> 1) * 8);
  }
  st.qa = opq(L_Q + w * 8192 + lane * 16);
  st.trg = w * 8192 + lane * 16;
  st.ost = opq(L_Q + ((2 * w + (qi >> 4)) * 256 + 32 * h5 + (qi & 15)) * 16);
  st.l0 = 0.f; st.l1 = 0.f; st.nm = splat16(0.f);
  st.s[0] = f32x16{}; st.s[1] = f32x16{}; st.pk[0] = u32x4{}; st.pk[1] = u32x4{};
#pragma unroll
  for (int k = 0; k < 8; ++k) st.vf[k] = bf16x8{};
#pragma unroll
  for (int d = 0; d < 4; ++d) st.o[d] = f32x16{};
  auto woff = [&](int i, int g, int SL) -> unsigned {
    const int k = 5 * g + 4 + SL;
    return k * WTILE + (k < 20 ? (h0 + 4 * ((i + 1) & 3)) * 256 : (h0 + 4 * ((i + 2) & 3)) * 256 - 20 * WTILE);
  };
  waitv<15>();
  bar();
  for (int g = 0; g < 4; ++g) {
    wdma(woff(-1, g, 0), 4); waitv<2>(); bar();
    wdma(woff(-1, g, 1), 0); waitv<2>(); bar();
    wdma(woff(-1, g, 2), 1); waitv<2>(); bar();
    wdma(woff(-1, g, 3), 2); wdma(woff(-1, g, 3) + WTILE, 3); waitv<4>(); bar();
  }
  for (int i = 0; i < 4; ++i) {
    const unsigned ob = (bh0 + 4 * (i - 1)) * OBH - 2048;
    const __amdgpu_buffer_rsrc_t r0 = o_rsrc(ows, i > 0);
    i32x4 sw = srdW; sw[2] = i < 3 ? srdW[2] : 0;
    st.tra = opq(st.qa - 2048);
    a_interval<0, 1, 1, true, true, 0, false, true>(st, r0, ob, sw, woff(i, 0, 0)); waitv<2>(); bar();
    a_interval<1, 1, 2, true, false, 1, false, true>(st, r0, ob, sw, woff(i, 0, 1)); waitv<2>(); bar();
    a_interval<2, 2, 1, true, false, 0, true, true>(st, r0, ob, sw, woff(i, 0, 2)); waitv<3>(); bar();
    a_interval<3, 1, 1, true, false, 0, true, true>(st, r0, ob, sw, woff(i, 0, 3)); waitv<6>(); bar();
    for (int g = 1; g < 4; ++g) {
      const __amdgpu_buffer_rsrc_t r1 = o_rsrc(ows, i > 0 && g < 3), r2 = o_rsrc(ows, i > 0 && g < 2);
      st.tra = opq(st.tra + 4096);
      a_interval<0, 1, 1, true, false, 0, true, true>(st, r1, ob + g * 4096, sw, woff(i, g, 0)); waitv<4>(); bar();
      a_interval<1, 1, 1, true, false, 0, true, true>(st, r1, ob + g * 4096, sw, woff(i, g, 1)); waitv<4>(); bar();
      a_interval<2, 1, 1, true, false, 0, true, true>(st, r2, ob + g * 4096, sw, woff(i, g, 2)); waitv<4>(); bar();
      a_interval<3, 1, 1, true, false, 0, true, true>(st, r2, ob + g * 4096, sw, woff(i, g, 3)); waitv<6>(); bar();
    }
  }
  const unsigned ob = (bh0 + 12) * OBH;
  const __amdgpu_buffer_rsrc_t ro = o_rsrc(ows, true);
  a_interval<0, 1, 1, false, false, 0, false, false>(st, ro, ob, srdW, 0);
  a_interval<1, 1, 0, false, false, 2, false, false>(st, ro, ob, srdW, 0);
  waitv<0>();
}

#define L_PG 4
#define L_G0 4
struct LState {
  f32x4 o8[8], s[2][2], nm;
  bf16x8 qf[4], vf[8];
  u32x4 pk;
  unsigned ka[4], va[8], qa, qw, tra, trg, dk[4], dv[2], pw[4];
  float l0, l1;
};
struct LDma { i32x4 sk, sv, sw; unsigned ok, ov, ow; };
template <int SL, int PV, int SM, bool QK, bool LQ, int EPI, bool TR, bool DMA>
DI void l_interval(LState& st, const LDma& dm, __amdgpu_buffer_rsrc_t ro, unsigned obase) {
  constexpr int X = (SL + 1) & 1, Y = SL & 1;
  constexpr unsigned VB = L_V + ((SL + 3) & 3) * 8192;
  bf16x8 kf[8], ot;
  float mx = 0.f, inv = 0.f;
  const bf16x8 pf = __builtin_bit_cast(bf16x8, st.pk);
  sfor<16>([&](auto gc) {
    constexpr int g = decltype(gc)::value;
    if constexpr (g < 8) {
      if constexpr (PV == 2) { f32x4 z = {}; st.o8[g] = MFMA16(st.vf[g], pf, z); }
      else if constexpr (PV == 1) st.o8[g] = MFMA16(st.vf[g], pf, st.o8[g]);
    } else if constexpr (QK) {
      constexpr int u = g & 1, i = (g - 8) >> 1;
      if constexpr (g == 8) __builtin_amdgcn_s_waitcnt(0xC87F);
      if constexpr (i == 0 && LQ) { f32x4 z = {}; st.s[Y][u] = MFMA16(kf[u], st.qf[0], z); }
      else if constexpr (i == 0) st.s[Y][u] = MFMA16(kf[u], st.qf[0], st.nm);
      else st.s[Y][u] = MFMA16(kf[2 * i + u], st.qf[i], st.s[Y][u]);
    }
    if constexpr (g < 4) {
      if constexpr (QK) { kf[2 * g] = lds_b128(st.ka[g] + (L_K + SL * 8192)); kf[2 * g + 1] = lds_b128(st.ka[g] + (L_K + SL * 8192 + 4096)); }
      if constexpr (LQ) st.qf[g] = lds_b128(st.qa + g * 1024);
      if constexpr (TR && g == 3) ot = lds_b128(st.tra + SL * 1024);
    }
    if constexpr (SM != 0) {
      const f32x4 &a = st.s[X][0], &c = st.s[X][1];
      if constexpr (g == 1) { mx = vmax3(a[0], a[1], a[2]); mx = vmax3(mx, a[3], c[0]); }
      if constexpr (g == 2) { mx = vmax3(mx, c[1], c[2]); mx = vmax3(mx, c[3], c[3]); }
      if constexpr (g >= 4 && g < 12) st.vf[g - 4] = lds_tr2(st.va[g - 4] + VB, st.va[g - 4] + (VB + 4096));
      if constexpr (g >= 8) {
        constexpr int n = (g - 8) >> 1, u = n >> 1, r = (n & 1) * 2;
        f32x4& s = st.s[X][u];
        if constexpr ((g & 1) == 0) { s[r] = __builtin_amdgcn_exp2f(s[r]); s[r + 1] = __builtin_amdgcn_exp2f(s[r + 1]); }
        else { st.l0 = vadd(st.l0, s[r]); st.l1 = vadd(st.l1, s[r + 1]); st.pk[n] = pk2(s[r], s[r + 1]); }
      }
    }
    if constexpr (DMA) {
      sfor<L_PG>([&](auto cc) {
        constexpr int n = L_PG * (g - L_G0) + decltype(cc)::value;
        constexpr unsigned hi = (n & 4) * 1024;
        if constexpr (n < 0) {}
        else if constexpr (n < 8) dmak<true, (n & 3) * 1024, (n & 3) == 0>(dm.sk, st.dk[n & 3], dm.ok + hi, L_K + ((SL + 3) & 3) * 8192 + hi);
        else if constexpr (n < 16) dmak<true, (n & 3) * 1024, (n & 3) == 0>(dm.sv, st.dv[n & 1], dm.ov + hi, L_V + ((SL + 2) & 3) * 8192 + hi);
      });
      if constexpr (g == 15) touch(dm.sw, st.pw[SL], dm.ow);
    }
    if constexpr (TR && g == 15) st128(ot, ro, st.trg, obase + SL * 1024);
    if constexpr (EPI != 0 && g >= 8) {
      constexpr int t = g - 8;
      u32x2 w = {pk2(vmul(st.o8[t][0], inv), vmul(st.o8[t][1], inv)), pk2(vmul(st.o8[t][2], inv), vmul(st.o8[t][3], inv))};
      if constexpr (EPI == 1) *(LDSP(u32x2))(st.qw + t * 512) = w;
      else __builtin_amdgcn_raw_buffer_store_b64(w, ro, st.qw - L_Q, obase + t * 512, O_AUX);
    }
    SB();
    if constexpr (g == 7) {
      if constexpr (EPI != 0) inv = __builtin_amdgcn_rcpf(swap32add(swap16add(st.l0 + st.l1)));
      if constexpr (SM == 2) {
        const float m = swap32max(swap16max(mx));
        st.nm = f32x4{-m, -m, -m, -m}; asm("" : "+v"(st.nm));
        st.l0 = 0.f; st.l1 = 0.f;
#pragma unroll
        for (int r = 0; r < 4; ++r) { st.s[X][0][r] = vadd(st.s[X][0][r], -m); st.s[X][1][r] = vadd(st.s[X][1][r], -m); }
      } else if constexpr (SM == 1) {
        if (__builtin_expect(__any(mx > THR2), 0)) {
          const float dl = fmaxf(swap32max(swap16max(mx)), 0.f);
          const float f = __builtin_amdgcn_exp2f(-dl);
          st.s[X][0] -= dl; st.s[X][1] -= dl; st.nm -= dl;
#pragma unroll
          for (int t = 0; t < 8; ++t) st.o8[t] *= f;
          st.l0 *= f; st.l1 *= f;
        }
      }
      SB();
    }
  });
}

DI void l_role(int lane, i32x4 srdK, i32x4 srdV, i32x4 srdW, unsigned b, unsigned h0, unsigned j, char* ows) {
  LState st;
  const unsigned c16 = lane & 15, g = lane >> 4, qq = (lane >> 2) & 3, pp = lane & 3, bh0 = b * NH + h0;
#pragma unroll
  for (int k = 0; k < 4; ++k) {
    st.ka[k] = opq(c16 * 256 + (((4 * k + g) ^ c16) << 4)); st.dk[k] = g * 256 + ((c16 ^ (4 * k + g)) << 4);
    st.pw[k] = pf_lane(lane, j, k);
  }
  st.dv[0] = g * 256 + ((c16 ^ (g << 2)) << 4); st.dv[1] = st.dv[0] ^ 32;
#pragma unroll
  for (int t = 0; t < 8; ++t)
    st.va[t] = opq((4 * g + qq) * 256 + ((((2 * t) ^ (qq << 2) ^ ((g & 1) << 1)) | (pp >> 1)) << 4) + (pp & 1) * 8);
  st.qa = opq(L_Q + 32768 + lane * 16);
  st.trg = 32768 + lane * 16;
  st.qw = opq(L_Q + 32768 + ((g >> 1) * 16 + c16) * 16 + (g & 1) * 8);
  st.l0 = 0.f; st.l1 = 0.f; st.pk = u32x4{}; st.nm = f32x4{};
  st.s[0][0] = f32x4{}; st.s[0][1] = f32x4{}; st.s[1][0] = f32x4{}; st.s[1][1] = f32x4{};
#pragma unroll
  for (int t = 0; t < 8; ++t) { st.vf[t] = bf16x8{}; st.o8[t] = f32x4{}; }
  auto toff = [&](int n) -> unsigned { return (bh0 + 4u * (unsigned)(n >> 4)) * (SK * 256u) + (unsigned)(n & 15) * 8192u; };
  auto dmv = [&](int i, int gq, int SL) -> LDma {
    LDma d;
    const int v = 16 * i + 4 * gq + SL, k = 5 * gq + 10;
    const unsigned wb0 = (h0 + 4 * ((i + 1) & 3)) * 256, wb1 = (h0 + 4 * ((i + 2) & 3)) * 256 - 20 * WTILE;
    d.sk = srdK; d.sv = srdV; d.sw = srdW;
    if (SL >= 1) d.sk[2] = v + 3 < 64 ? srdK[2] : 0;
    if (SL >= 2) d.sv[2] = v + 2 < 64 ? srdV[2] : 0;
    d.ok = toff(v + 3); d.ov = toff(v + 2);
    d.ow = k * WTILE + (k < 20 ? wb0 : wb1);
    return d;
  };
  if (lane < 8) ((unsigned*)(ows + F_OFF))[(blockIdx.x * 8 + lane) * 64] = 0;
  bar();
  for (int n = 0; n < 10; ++n) {
    const bool v = n >= 6;
    const unsigned so = toff(0) + (v ? n - 6 : n) * 4096, la = (v ? L_V + (n - 6) * 4096 : L_K + n * 4096);
    const i32x4 sr = v ? srdV : srdK;
    dmak<true, 0, true>(sr, v ? st.dv[0] : st.dk[0], so, la);
    dmak<true, 1024, false>(sr, v ? st.dv[1] : st.dk[1], so, 0);
    dmak<true, 2048, false>(sr, v ? st.dv[0] : st.dk[2], so, 0);
    dmak<true, 3072, false>(sr, v ? st.dv[1] : st.dk[3], so, 0);
    bar();
  }
#pragma unroll
  for (int k = 0; k < 8; ++k) touch(srdW, st.pw[k & 3], (k >> 2) * 5 * WTILE + (h0 + 4) * 256);
  for (int n = 10; n < 15; ++n) bar();
  waitv<0>();
  bar();
  for (int i = 0; i < 4; ++i) {
    const unsigned ob = (bh0 + 4 * (i - 1)) * OBH - 2048;
    const __amdgpu_buffer_rsrc_t r0 = o_rsrc(ows, i > 0);
    st.tra = opq(st.qa - 2048);
    l_interval<0, 1, 1, true, true, 0, false, true>(st, dmv(i, 0, 0), r0, ob); waitv<34>(); bar();
    l_interval<1, 1, 2, true, false, 1, false, true>(st, dmv(i, 0, 1), r0, ob); waitv<34>(); bar();
    l_interval<2, 2, 1, true, false, 0, true, true>(st, dmv(i, 0, 2), r0, ob); waitv<34>(); bar();
    l_interval<3, 1, 1, true, false, 0, true, true>(st, dmv(i, 0, 3), r0, ob); waitv<34>(); bar();
    st.tra = opq(st.qa + 2048);
    for (int gq = 1; gq < 4; ++gq) {
      const __amdgpu_buffer_rsrc_t r1 = o_rsrc(ows, i > 0 && gq < 2);
      l_interval<0, 1, 1, true, false, 0, true, true>(st, dmv(i, gq, 0), r1, ob + 4096); waitv<34>(); bar();
      l_interval<1, 1, 1, true, false, 0, true, true>(st, dmv(i, gq, 1), r1, ob + 4096); waitv<34>(); bar();
      l_interval<2, 1, 1, true, false, 0, false, true>(st, dmv(i, gq, 2), r1, ob); waitv<34>(); bar();
      l_interval<3, 1, 1, true, false, 0, false, true>(st, dmv(i, gq, 3), r1, ob); waitv<34>(); bar();
    }
  }
  const unsigned ob = (bh0 + 12) * OBH;
  const __amdgpu_buffer_rsrc_t ro = o_rsrc(ows, true);
  l_interval<0, 1, 1, false, false, 0, false, false>(st, dmv(0, 0, 0), ro, ob);
  l_interval<1, 1, 0, false, false, 2, false, false>(st, dmv(0, 0, 0), ro, ob);
  waitv<0>();
}

#define P_LA 5
struct PState {
  f32x4 acc[3][8];
  bf16x8 xs[2][3][2], xc[2][3], wf[10];
  unsigned wa[8], xo, xw[2], xr[2], qw[3], q2s;
  i32x4 sx;
};
DI void p_rdw(PState& st, int it) {
  const int sl = (it >> 3) % 5, t = it & 7;
  st.wf[it % 10] = lds_tr2(st.wa[t] + sl * 8192, st.wa[t] + (sl * 8192 + 1024));
}
DI void p_ldx(PState& st, int D, int n) { ldx(st.xs[D & 1][n >> 1][n & 1], st.sx, st.xo, (D % 10) * 128 + n * (8 * DM * 2)); }
DI void p_wrx(PState& st, int D, int n) { *(LDSP(bf16x8))(st.xw[n & 1] + (n >> 1) * 2048) = st.xs[D & 1][n >> 1][n & 1]; }
DI void p_handq(PState& st, int t) {
#pragma unroll
  for (int q = 0; q < 3; ++q) {
    const f32x4 a = st.acc[q][t];
    u32x2 w = {pk2(vmul(a[0], CSC), vmul(a[1], CSC)), pk2(vmul(a[2], CSC), vmul(a[3], CSC))};
    *(LDSP(u32x2))(q < 2 ? st.qw[q] + t * 1024 : st.qw[2] + t * st.q2s) = w;
  }
}
DI void p_item(PState& st, int it) {
  const int ks = it >> 3, t = it & 7, D = it >> 4, u = it & 15;
#pragma unroll
  for (int q = 0; q < 3; ++q) {
    if (ks == 0) { f32x4 z = {}; st.acc[q][t] = MFMA16(st.wf[it % 10], st.xc[0][q], z); }
    else st.acc[q][t] = MFMA16(st.wf[it % 10], st.xc[ks & 1][q], st.acc[q][t]);
  }
  p_rdw(st, it + P_LA);
  if (u == 0 || u == 8) {
#pragma unroll
    for (int q = 0; q < 3; ++q) st.xc[u == 0][q] = lds_b128(st.xr[u == 0] + q * 2048);
  }
  if (u == 1) waitv<11>();
  if (u == 2 || u == 3) waitv<10>();
  if (u == 4 || u == 5) waitv<9>();
  if (u == 6) waitv<8>();
  if (u >= 1 && u < 7) p_wrx(st, D + 1, u - 1);
  if (u >= 2 && u < 14 && (u & 1) == 0) p_ldx(st, D + 3, (u - 2) >> 1);
  if (ks == 19 && t >= 1) p_handq(st, t - 1);
}

DI void p_role(int p, int lane, i32x4 srdX, unsigned b, const char* wo) {
  PState st;
  const unsigned c16 = lane & 15, g = lane >> 4, qq = (lane >> 2) & 3, pp = lane & 3, qb0 = 3 * (p - 1);
  st.xo = (b * SQ + 16 * qb0 + (lane >> 3)) * (DM * 2) + (lane & 7) * 16;
  st.sx = srdX;
#pragma unroll
  for (int n = 0; n < 12; ++n) { st.xs[n / 6][(n % 6) >> 1][n & 1] = bf16x8{}; p_ldx(st, n / 6, n % 6); }
#pragma unroll
  for (int t = 0; t < 8; ++t)
    st.wa[t] = opq(L_W + (8 * g + qq) * 256 + ((((2 * t) ^ (qq << 2) ^ ((g & 1) << 1)) | (pp >> 1)) << 4) + (pp & 1) * 8);
  st.xw[0] = opq(L_X + qb0 * 2048 + (lane >> 3) * 128 + (((lane & 7) ^ (lane >> 4)) << 4));
  st.xw[1] = opq(L_X + qb0 * 2048 + 1024 + (lane >> 3) * 128 + (((lane & 7) ^ (lane >> 4) ^ 4) << 4));
  st.xr[0] = opq(L_X + qb0 * 2048 + c16 * 128 + ((g ^ (c16 >> 1)) << 4));
  st.xr[1] = opq(st.xr[0] ^ 64);
#pragma unroll
  for (int q = 0; q < 3; ++q) {
    const unsigned qb = qb0 + q;
    st.qw[q] = opq(qb == 8 ? L_Q + 32768 + ((g >> 1) * 16 + c16) * 16 + (g & 1) * 8
                           : L_Q + ((qb >> 1) * 512 + (g >> 1) * 32 + (qb & 1) * 16 + c16) * 16 + (g & 1) * 8);
  }
  st.q2s = p == 3 ? 512 : 1024;
  waitv<6>(); SB();
#pragma unroll
  for (int q = 0; q < 3; ++q) { p_wrx(st, 0, 2 * q); p_wrx(st, 0, 2 * q + 1); st.xc[0][q] = lds_b128(st.xr[0] + q * 2048); }
  SB();
#pragma unroll
  for (int n = 0; n < 6; ++n) p_ldx(st, 2, n);
  bar();
#pragma unroll
  for (int it = 0; it < P_LA; ++it) p_rdw(st, it);
  for (int i = -1; i < 3; ++i) {
    sfor<16>([&](auto IVc) {
      constexpr int IV = decltype(IVc)::value;
#pragma unroll
      for (int it = 10 * IV; it < 10 * IV + 10; ++it) { p_item(st, it); SB(); }
      if constexpr (IV == 15) { p_handq(st, 7); bar(); } else bar<2 * (P_LA - 2)>();
    });
  }
  waitv<0>(); SB();
  const i32x4 so = mk_srd(wo, NH * DH * DM * 2);
  for (int v = 0; v < 16; ++v) {
    if (p < 3 && v < 10) dma1<false, 0>(so, lane * 16, (blockIdx.x * 20 + (p - 1) * 10 + v) * 1024, L_D);
    bar();
  }
  waitv<0>();
}

__global__ __launch_bounds__(512, 2) void attn_fused(const char* __restrict__ x, const char* __restrict__ kc, const char* __restrict__ vc,
                                                     const char* __restrict__ wq, char* __restrict__ ows, const char* __restrict__ wo) {
  const int wid = __builtin_amdgcn_readfirstlane(threadIdx.x >> 6), lane = threadIdx.x & 63;
  const unsigned w = blockIdx.x, xcd = w & 7, j = w >> 3, b = 8 * (xcd >> 1) + (j >> 2), h0 = 16 * (xcd & 1) + (j & 3);
  const i32x4 srdW = mk_srd(wq, DM * NH * DH * 2), srdX = mk_srd(x, NB * SQ * DM * 2);
  const i32x4 srdK = mk_srd(kc, NB * NH * SK * DH * 2), srdV = mk_srd(vc, NB * NH * SK * DH * 2);
  if (wid < 4) {
    a_role(wid, lane, srdX, srdW, b, h0, j, ows);
  } else {
    if (wid == 4) __builtin_amdgcn_s_setprio(1);
    if (wid == 4) l_role(lane, srdK, srdV, srdW, b, h0, j, ows);
    else p_role(wid - 4, lane, srdX, b, wo);
  }
}

#define K2_LA 3
constexpr unsigned K2_LDS = 163840;
constexpr unsigned K2_XB = 3328;
#define K2_FS 10
#define K2_SL 2

__global__ __launch_bounds__(512, 2) void out_proj(char* __restrict__ ows, const char* __restrict__ wo, char* __restrict__ out) {
  const int wid = __builtin_amdgcn_readfirstlane(threadIdx.x >> 6), lane = threadIdx.x & 63;
  const unsigned xcd = blockIdx.x & 7, j = blockIdx.x >> 3, kh = xcd & 1, rt = 6 * (xcd >> 1) + j / 5, ct = j % 5;
  const unsigned qg = wid & 3, kg = wid >> 2;
  const unsigned c16 = lane & 15, g = lane >> 4, qq = (lane >> 2) & 3, pp = lane & 3;
  unsigned wa[8], xo[3];
  const unsigned sw = 2 * kg + 4 * kh;
#pragma unroll
  for (int t = 0; t < 8; ++t)
    wa[t] = kg * 8192 + (8 * g + qq) * 256 + ((((2 * (t ^ sw)) ^ (qq << 2) ^ ((g & 1) << 1)) | (pp >> 1)) << 4) + (pp & 1) * 8;
#pragma unroll
  for (int q = 0; q < 3; ++q) {
    const unsigned Qb = 12 * rt + 3 * qg + q, bb = Qb / 9, qi = Qb % 9;
    xo[q] = (bb * NH + kh * 16) * OBH + qi * 4096 + kg * 1024 + lane * 16;
  }
  const i32x4 srdW = mk_srd(wo, NH * DH * DM * 2);
  const i32x4 ro = mk_srd(ows, NB * SQ * NH * DH * 2);
  const unsigned dw = (kh * 2048 + 8 * wid + g) * (DM * 2) + ct * 256 + ((c16 ^ (g << 2) ^ ((wid & 1) << 1)) << 4);
  f32x4 acc[3][8];
#pragma unroll
  for (int q = 0; q < 3; ++q)
#pragma unroll
    for (int t = 0; t < 8; ++t) acc[q][t] = f32x4{};
  bf16x8 xf[4][3] = {}, wf[8];
  auto piece = [&](int k, unsigned s, int slot) {
    const unsigned so = s * (64u * DM * 2);
    if (k == 0) dma1<false, 0>(srdW, dw, so, slot * 16384 + wid * 2048);
    else if (k == 1) dma1<false, 1024>(srdW, dw, so + 4 * DM * 2 - 1024, slot * 16384 + wid * 2048);
    else ldx(xf[slot][k - 2], ro, xo[k - 2], (s >> 1) * OBH + (s & 1) * 2048);
  };
  auto rdw = [&](int slot, int t) { wf[t] = lds_tr2(wa[t] + slot * 16384, wa[t] + slot * 16384 + 1024); };
#pragma unroll
  for (int s = 0; s < 3; ++s) {
#pragma unroll
    for (int k = 0; k < 5; ++k) piece(k, s, s);
    if (s < 2) __builtin_amdgcn_s_barrier();
  }
  waitv<8>();
  bar();
#pragma unroll
  for (int t = 0; t < K2_LA; ++t) rdw(0, t);
  for (int s0 = 0; s0 < 28; s0 += 4) sfor<4>([&](auto uc) {
    constexpr int u = decltype(uc)::value;
#pragma unroll
    for (int t = 0; t < 8; ++t) {
      acc[0][t] = MFMA16(wf[t], xf[u][0], acc[0][t]);
      SB();
      rdw((u + (t + K2_LA) / 8) & 3, (t + K2_LA) & 7);
      SB();
      acc[1][t] = MFMA16(wf[t], xf[u][1], acc[1][t]);
      if (t < 5) piece(t, s0 + u + 3, (u + 3) & 3);
      acc[2][t] = MFMA16(wf[t], xf[u][2], acc[2][t]);
      SB();
    }
    waitv<8>();
    bar<2 * K2_LA>();
    SB();
  });
  const unsigned rw = opq(65536 + wid * 12288 + lane * 16), rr = opq(65536 + (wid ^ 4) * 12288 + lane * 16);
  constexpr auto tl = [](int i) { return ((i & 3) ^ ((i & 12) == 12 ? 2 : 0)) + (i >> 4) * 4; };
  auto tail = [&](auto ic) {
    constexpr int i = decltype(ic)::value, u = (i >> 2) & 3, t = tl(i), n = i + K2_LA;
    acc[0][t] = MFMA16(wf[i & 7], xf[u][0], acc[0][t]);
    SB();
    if (n < 32) {
      const unsigned a = wa[tl(n)] + ((n >> 2) & 3) * 16384;
      wf[n & 7] = lds_tr2(a, a + 1024);
    }
    if (i < 2) piece(2 * i, 31, 3);
    SB();
    acc[1][t] = MFMA16(wf[i & 7], xf[u][1], acc[1][t]);
    if (i < 3) piece(i < 2 ? 2 * i + 1 : 4, 31, 3);
    acc[2][t] = MFMA16(wf[i & 7], xf[u][2], acc[2][t]);
    SB();
    if ((i & 14) == 14) {
#pragma unroll
      for (int q = 0; q < 3; ++q) *(LDSP(f32x4))(rw + (i >> 4) * 6144 + (2 * q + (i & 1)) * 1024) = acc[q][t + 2];
    }
    if (i == 3) { waitv<8>(); bar<2 * K2_LA>(); }
    if (i == 7) { waitv<3>(); bar<2 * K2_LA>(); }
    if (i == 11) waitv<0>();
    SB();
  };
  sfor<16>(tail);
  const __amdgpu_buffer_rsrc_t rx = __builtin_amdgcn_make_buffer_rsrc((void*)(ows + X_OFF), 0, 240 * 8 * K2_XB, 0x00020000);
  unsigned* flags = (unsigned*)(ows + F_OFF);
  bar();
  unsigned ex = 0;
#pragma unroll
  for (int n = 0; n < 6; ++n) acc[n >> 1][n & 1] += *(LDSP(f32x4))(rr + n * 1024);
#pragma unroll
  for (int q = 0; q < 3; ++q) {
    const f32x4 y0 = acc[q][0], y1 = acc[q][1];
    unsigned mb = 0;
#pragma unroll
    for (int i = 0; i < 4; ++i) mb = max(mb, max(__float_as_uint(y0[i]) & 0x7fffffffu, __float_as_uint(y1[i]) & 0x7fffffffu));
    const unsigned e = mb >> 23;
    const float inv = __uint_as_float((253u - min(e, 252u)) << 23);
    auto pk = [&](float x, float y) { return __builtin_bit_cast(unsigned, __builtin_amdgcn_cvt_pknorm_i16(x * inv, y * inv)); };
    const u32x4 w = {pk(y0[0], y0[1]), pk(y0[2], y0[3]), pk(y1[0], y1[1]), pk(y1[2], y1[3])};
    st128<16>(__builtin_bit_cast(bf16x8, w), rx, lane * 16 + q * 1024, (blockIdx.x * 8 + wid) * K2_XB);
    ex |= e << (8 * q);
  }
  __builtin_amdgcn_raw_buffer_store_b32(ex, rx, 3072 + lane * 4, (blockIdx.x * 8 + wid) * K2_XB, 16);
  SB();
  sfor<K2_FS>([&](auto ic) { tail(std::integral_constant<int, 16 + decltype(ic)::value>{}); });
  waitv<0>();
  if (lane == 0) __hip_atomic_store(flags + (blockIdx.x * 8 + wid) * 64, 1u, __ATOMIC_RELAXED, __HIP_MEMORY_SCOPE_AGENT);
  SB();
  sfor<16 - K2_FS>([&](auto ic) { tail(std::integral_constant<int, 16 + K2_FS + decltype(ic)::value>{}); });
  const unsigned id = (blockIdx.x ^ 1) * 8 + wid;
  while (__hip_atomic_load(flags + id * 64, __ATOMIC_RELAXED, __HIP_MEMORY_SCOPE_AGENT) == 0) __builtin_amdgcn_s_sleep(K2_SL);
  asm volatile("" ::: "memory");
  u32x4 r[3];
  ex = __builtin_amdgcn_raw_buffer_load_b32(rx, 3072 + lane * 4, id * K2_XB, 16);
#pragma unroll
  for (int q = 0; q < 3; ++q) r[q] = __builtin_amdgcn_raw_buffer_load_b128(rx, lane * 16 + q * 1024, id * K2_XB, 16);
  bar();
  f32x4 v[6];
#pragma unroll
  for (int n = 0; n < 6; ++n) v[n] = acc[n >> 1][4 + (n & 1)] + *(LDSP(f32x4))(rr + 6144 + n * 1024);
#pragma unroll
  for (int q = 0; q < 3; ++q) {
    const unsigned e = (ex >> (8 * q)) & 255;
    const float sc = __uint_as_float(e > 252 ? 0x7fc00000u : (e + 1) << 23) * (1.0f / 32767.0f);
    f32x4 y0, y1;
#pragma unroll
    for (int i = 0; i < 4; ++i) {
      y0[i] = __builtin_fmaf((float)(short)(r[q][i >> 1] >> (16 * (i & 1))), sc, v[2 * q][i]);
      y1[i] = __builtin_fmaf((float)(short)(r[q][2 + (i >> 1)] >> (16 * (i & 1))), sc, v[2 * q + 1][i]);
    }
    const auto r0 = __builtin_amdgcn_permlane16_swap(pk2(y0[0], y0[1]), pk2(y1[0], y1[1]), false, false);
    const auto r1 = __builtin_amdgcn_permlane16_swap(pk2(y0[2], y0[3]), pk2(y1[2], y1[3]), false, false);
    const u32x4 y = {r0[0], r1[0], r0[1], r1[1]};
    const unsigned row = 192 * rt + 48 * qg + 16 * q + c16, col = 128 * ct + 16 * (sw ^ 4) + 16 * (g & 1) + 8 * (g >> 1);
    *(u32x4*)(out + (row * DM + col) * 2) = y;
  }
}

extern "C" void kernel_launch(void* const* d_in, const int* in_sizes, int n_in, void* d_out, int out_size, void* d_ws, size_t ws_size,
                              hipStream_t stream) {
  (void)in_sizes; (void)n_in; (void)out_size; (void)ws_size;
  (void)hipFuncSetAttribute((const void*)attn_fused, hipFuncAttributeMaxDynamicSharedMemorySize, (int)L_END);
  (void)hipFuncSetAttribute((const void*)out_proj, hipFuncAttributeMaxDynamicSharedMemorySize, (int)K2_LDS);
  attn_fused<<<256, 512, L_END, stream>>>((const char*)d_in[0], (const char*)d_in[1], (const char*)d_in[2], (const char*)d_in[3], (char*)d_ws, (const char*)d_in[4]);
  out_proj<<<240, 512, K2_LDS, stream>>>((char*)d_ws, (const char*)d_in[4], (char*)d_out);
}
```
